```python
import torch

def _softmax(x, axis=-1):
    return torch.softmax(x, dim=axis)
def _gelu(x):
    return torch.nn.functional.gelu(x, approximate="tanh")
def _silu(x):
    return torch.nn.functional.silu(x)
def _sigmoid(x):
    return torch.sigmoid(x)
def _layernorm(x, axis=-1, eps=1e-5):
    m = x.mean(dim=axis, keepdim=True)
    v = x.var(dim=axis, keepdim=True, unbiased=False)
    return (x - m) / torch.sqrt(v + eps)
def _scatter_add(tgt, idx, src, axis=0):
    return tgt.clone().index_add_(axis, idx, src)
def _segment_sum(src, seg, n):
    out = torch.zeros((n,) + src.shape[1:], dtype=src.dtype)
    return out.index_add_(0, seg, src)
def _pad(x, pw):
    flat = tuple(v for pair in reversed(pw) for v in pair)
    return torch.nn.functional.pad(x, flat)

kernel_name = 'interleave_layers__gqa_block_cached__mixer_token_mlp__decode'

def setup_inputs():

    _w = (lambda a: (lambda t: t.to(torch.bfloat16) if t.is_floating_point() else t)(torch.as_tensor(a)))
    def _A_setup():
        B, Sq, D = 8, 1, 448
        Hq, Hkv, Sk, Dh = 16, 8, 8192, 256
        assert Hq % Hkv == 0, (Hq, Hkv)
        return {
            "x": _w((lambda *s: torch.randn(*s, dtype=torch.bfloat16) * 0.05)(B, Sq, D)),
            "k_cache": _w((lambda *s: torch.randn(*s, dtype=torch.bfloat16) * 0.8)(B, Hkv, Sk, Dh)),
            "v_cache": _w((lambda *s: torch.randn(*s, dtype=torch.bfloat16) * 1.6)(B, Hkv, Sk, Dh)),
            "wq": _w((lambda *s: torch.randn(*s, dtype=torch.bfloat16) * 0.8)(D, Hq * Dh) * (D ** -0.5)),
            "wo": _w((lambda *s: torch.randn(*s, dtype=torch.bfloat16) * 1.6)(Hq * Dh, D) * ((Hq * Dh) ** -0.5)),
        }
    _A_in = _A_setup()

    def _B_setup():
        B, S, D, F = 8, 1, 448, 1792
        return {
            "x": _w((lambda *s: torch.randn(*s, dtype=torch.bfloat16) * 0.05)(B, S, D)),
            "w1": _w((lambda *s: torch.randn(*s, dtype=torch.bfloat16) * 0.8)(S, F) * (S ** -0.5)),
            "b1": _w((lambda *s: torch.randn(*s, dtype=torch.bfloat16) * 0.8)(F) * 0.02),
            "w2": _w((lambda *s: torch.randn(*s, dtype=torch.bfloat16) * 0.2)(F, S) * (F ** -0.5)),
            "b2": _w((lambda *s: torch.randn(*s, dtype=torch.bfloat16) * 0.2)(S) * 0.02),
        }
    _B_in = _B_setup()

    return {
        "A_x": _A_in["x"],
        "A_k_cache": _A_in["k_cache"],
        "A_v_cache": _A_in["v_cache"],
        "A_wq": _A_in["wq"],
        "A_wo": _A_in["wo"],
        "B_x": _B_in["x"],
        "B_w1": _B_in["w1"],
        "B_b1": _B_in["b1"],
        "B_w2": _B_in["w2"],
        "B_b2": _B_in["b2"],
    }

def reference(A_x, A_k_cache, A_v_cache, A_wq, A_wo, B_x, B_w1, B_b1, B_w2, B_b2):
    def _A_ref(x, k_cache, v_cache, wq, wo):
        B, Sq, D = x.shape
        Hq, Dh = 16, 256
        Hkv = k_cache.shape[1]
        rep = Hq // Hkv
        q = (lambda x, *d: x.permute(*d))((x @ wq).reshape(B, Sq, Hq, Dh), 0, 2, 1, 3)
        k = (lambda x, n, axis: torch.repeat_interleave(x, n, dim=axis))(k_cache, rep, axis=1)
        v = (lambda x, n, axis: torch.repeat_interleave(x, n, dim=axis))(v_cache, rep, axis=1)
        scores = torch.einsum("bhqd,bhkd->bhqk", q, k) * (Dh ** -0.5)
        p = _softmax(scores, axis=-1)
        o = torch.einsum("bhqk,bhkd->bhqd", p, v)
        return (lambda x, *d: x.permute(*d))(o, 0, 2, 1, 3).reshape(B, Sq, Hq * Dh) @ wo

    def _B_ref(x, w1, b1, w2, b2):
        B, S, D = x.shape
        h = _layernorm(x, axis=-1)
        u = torch.einsum("bsd,sf->bdf", h, w1) + b1
        u = _gelu(u)
        y = torch.einsum("bdf,fs->bds", u, w2) + b2
        return (lambda x, *d: x.permute(*d))(y, 0, 2, 1)

    x = A_x
    x = x + _A_ref(_layernorm(x, axis=-1), A_k_cache, A_v_cache, A_wq, A_wo)
    x = x + _B_ref(_layernorm(x, axis=-1), B_w1, B_b1, B_w2, B_b2)
    x = x + _A_ref(_layernorm(x, axis=-1), A_k_cache, A_v_cache, A_wq, A_wo)
    x = x + _B_ref(_layernorm(x, axis=-1), B_w1, B_b1, B_w2, B_b2)
    return x
```

```cpp
#include <hip/hip_runtime.h>
#include <hip/hip_bf16.h>
typedef unsigned short u16;
typedef unsigned u32x4 __attribute__((ext_vector_type(4)));
__device__ __forceinline__ float bf2f(u16 x){ return __uint_as_float((unsigned)x<<16); }
__device__ __forceinline__ u16 f2bf(float f){ __hip_bfloat16 h=__float2bfloat16(f); return *(u16*)&h; }
__device__ __forceinline__ float wsum(float v){ for(int o=32;o>0;o>>=1) v+=__shfl_xor(v,o); return v; }
__device__ __forceinline__ float wmax(float v){ for(int o=32;o>0;o>>=1) v=fmaxf(v,__shfl_xor(v,o)); return v; }
constexpr int NS=32;
__global__ void k_q(const float* x,const u16* wq,float* q){
  __shared__ float u[448];
  int b=blockIdx.y,h=blockIdx.x,t=threadIdx.x;
  if(t<64){ float v[7],s=0; for(int i=0;i<7;++i){v[i]=x[b*448+t+64*i]; s+=v[i];} float m=wsum(s)/448; float ss=0; for(int i=0;i<7;++i) ss+=(v[i]-m)*(v[i]-m); float r=rsqrtf(wsum(ss)/448+1e-5f); for(int i=0;i<7;++i) u[t+64*i]=(v[i]-m)*r; }
  __syncthreads();
  float a=0; for(int k=0;k<448;++k) a+=u[k]*bf2f(wq[k*4096+h*256+t]);
  q[b*4096+h*256+t]=bf2f(f2bf(a));
}
__global__ void k_init(const u16* x,float* xf){ int i=blockIdx.x*256+threadIdx.x; if(i<8*448) xf[i]=bf2f(x[i]); }
__global__ void __launch_bounds__(256) k_attn(const float* q,const u16* K,const u16* V,float* part){
  __shared__ float sc[2][256]; __shared__ float red[4][2][256]; __shared__ float ml[4];
  int s=blockIdx.x,g=blockIdx.y,b=blockIdx.z,t=threadIdx.x,lane=t&63,w=t>>6;
  const u16* Kp=K+((size_t)(b*8+g)*8192+s*256)*256; const u16* Vp=V+((size_t)(b*8+g)*8192+s*256)*256;
  float q0[8],q1[8]; int c=lane&31; for(int j=0;j<8;++j){q0[j]=q[b*4096+(2*g)*256+8*c+j]; q1[j]=q[b*4096+(2*g+1)*256+8*c+j];}
  for(int i=0;i<32;i+=4){ u32x4 kv[4];
    for(int u=0;u<4;++u) kv[u]=__builtin_nontemporal_load((const u32x4*)(Kp+(size_t)(w*64+(i+u)*2)*256)+lane);
    for(int u=0;u<4;++u){ float a0=0,a1=0; for(int j=0;j<4;++j){ float lo=__uint_as_float(kv[u][j]<<16),hi=__uint_as_float(kv[u][j]&0xffff0000u); a0+=lo*q0[2*j]+hi*q0[2*j+1]; a1+=lo*q1[2*j]+hi*q1[2*j+1]; }
      for(int o=16;o>0;o>>=1){a0+=__shfl_xor(a0,o);a1+=__shfl_xor(a1,o);} if(c==0){int key=w*64+(i+u)*2+(lane>>5); sc[0][key]=a0*0.0625f; sc[1][key]=a1*0.0625f;} } }
  __syncthreads();
  if(w<2){ float v[4],m=-1e30f; for(int i=0;i<4;++i){v[i]=sc[w][lane+64*i]; m=fmaxf(m,v[i]);} m=wmax(m); float l=0; for(int i=0;i<4;++i){v[i]=__expf(v[i]-m); l+=v[i]; sc[w][lane+64*i]=v[i];} l=wsum(l); if(lane==0){ml[2*w]=m;ml[2*w+1]=l;} }
  __syncthreads();
  float a0[8]={0},a1[8]={0};
  for(int i=0;i<32;i+=4){ u32x4 vv[4];
    for(int u=0;u<4;++u) vv[u]=__builtin_nontemporal_load((const u32x4*)(Vp+(size_t)(w*64+(i+u)*2)*256)+lane);
    for(int u=0;u<4;++u){ int key=w*64+(i+u)*2+(lane>>5); float p0=sc[0][key],p1=sc[1][key]; for(int j=0;j<4;++j){ float lo=__uint_as_float(vv[u][j]<<16),hi=__uint_as_float(vv[u][j]&0xffff0000u); a0[2*j]+=p0*lo;a0[2*j+1]+=p0*hi;a1[2*j]+=p1*lo;a1[2*j+1]+=p1*hi; } } }
  for(int j=0;j<8;++j){a0[j]+=__shfl_xor(a0[j],32);a1[j]+=__shfl_xor(a1[j],32);}
  if(lane<32) for(int j=0;j<8;++j){red[w][0][8*c+j]=a0[j];red[w][1][8*c+j]=a1[j];}
  __syncthreads();
  for(int h=0;h<2;++h){ float* p=part+((size_t)(b*16+2*g+h)*NS+s)*260; p[t]=red[0][h][t]+red[1][h][t]+red[2][h][t]+red[3][h][t]; if(t==0){p[256]=ml[2*h];p[257]=ml[2*h+1];} }
}
__global__ void k_comb(const float* part,const u16* wo,float* y){
  __shared__ float o[256];
  int h=blockIdx.x,b=blockIdx.y,t=threadIdx.x; const float* p=part+(size_t)(b*16+h)*NS*260;
  float M=-1e30f; for(int s=0;s<NS;++s) M=fmaxf(M,p[s*260+256]);
  float L=0,a=0; for(int s=0;s<NS;++s){ float w=__expf(p[s*260+256]-M); L+=w*p[s*260+257]; a+=w*p[s*260+t]; }
  o[t]=a/L; __syncthreads();
  for(int n=t;n<448;n+=256){ float acc=0; for(int k=0;k<256;++k) acc+=o[k]*bf2f(wo[(size_t)(h*256+k)*448+n]); y[(b*16+h)*448+n]=acc; }
}
__global__ void k_mix(const float* xin,const float* y,const u16* w1,const u16* b1,const u16* w2,const u16* b2,float* xout,u16* out){
  __shared__ float x1[448]; __shared__ float st[2];
  int b=blockIdx.y,t=threadIdx.x,lane=t&63,w=t>>6;
  for(int n=t;n<448;n+=256){ float a=xin[b*448+n]; for(int h=0;h<16;++h) a+=y[(b*16+h)*448+n]; x1[n]=a; }
  __syncthreads();
  if(t<64){ float s=0; for(int i=0;i<7;++i) s+=x1[t+64*i]; float m=wsum(s)/448; float ss=0; for(int i=0;i<7;++i){float d=x1[t+64*i]-m; ss+=d*d;} float v=wsum(ss)/448; float r=rsqrtf(v+1e-5f); float v2=v*r*r; r*=rsqrtf(v2+1e-5f); if(t==0){st[0]=m;st[1]=r;} }
  __syncthreads();
  for(int j=0;j<2;++j){ int d=blockIdx.x*8+w*2+j; float h=bf2f(f2bf((x1[d]-st[0])*st[1])); float a=0;
    for(int f=lane;f<1792;f+=64){ float u=h*bf2f(w1[f])+bf2f(b1[f]); float z=1.5957691216f*(u+0.044715f*u*u*u); a+=u/(1.f+__expf(-z))*bf2f(w2[f]); }
    a=wsum(a)+bf2f(b2[0]); if(lane==0){ float r=x1[d]+a; xout[b*448+d]=r; if(out) out[b*448+d]=f2bf(r);} }
}
extern "C" void kernel_launch(void* const* d_in, const int* in_sizes, int n_in, void* d_out, int out_size, void* d_ws, size_t ws_size, hipStream_t stream){
  const u16* x=(const u16*)d_in[0]; const u16* K=(const u16*)d_in[1]; const u16* V=(const u16*)d_in[2]; const u16* wq=(const u16*)d_in[3]; const u16* wo=(const u16*)d_in[4];
  const u16* w1=(const u16*)d_in[6]; const u16* b1=(const u16*)d_in[7]; const u16* w2=(const u16*)d_in[8]; const u16* b2=(const u16*)d_in[9];
  float* ws=(float*)d_ws; float* x0=ws; float* x2=ws+4096; float* x4=ws+8192; float* q=ws+16384; float* y=q+32768; float* part=y+65536;
  k_init<<<14,256,0,stream>>>(x,x0);
  k_q<<<dim3(16,8),256,0,stream>>>(x0,wq,q);
  k_attn<<<dim3(NS,8,8),256,0,stream>>>(q,K,V,part);
  k_comb<<<dim3(16,8),256,0,stream>>>(part,wo,y);
  k_mix<<<dim3(56,8),256,0,stream>>>(x0,y,w1,b1,w2,b2,x2,nullptr);
  k_q<<<dim3(16,8),256,0,stream>>>(x2,wq,q);
  k_attn<<<dim3(NS,8,8),256,0,stream>>>(q,K,V,part);
  k_comb<<<dim3(16,8),256,0,stream>>>(part,wo,y);
  k_mix<<<dim3(56,8),256,0,stream>>>(x2,y,w1,b1,w2,b2,x4,(u16*)d_out);
}
```

```cpp
#include <hip/hip_runtime.h>

typedef unsigned short u16;
typedef unsigned int u32;
typedef unsigned long long u64;
typedef u32 u32x2 __attribute__((ext_vector_type(2)));
typedef u32 u32x4 __attribute__((ext_vector_type(4)));
typedef float f32x2 __attribute__((ext_vector_type(2)));
typedef float f32x4 __attribute__((ext_vector_type(4)));
typedef __bf16 bf16x2 __attribute__((ext_vector_type(2)));
#define DI __device__ __forceinline__
#define LAS __attribute__((address_space(3)))
#define GAS __attribute__((address_space(1)))

#define PROF 0
#define STANDALONE 0

constexpr int NCHAIN = 16, NSPLIT = 30, WKEYS = 68;
constexpr int TAB_E0 = 109, TAB_NB = 22, TAB_N = 2 * TAB_NB * 128;
constexpr u32 WS_Q = 0;
constexpr u32 WS_TAB = 131072;
constexpr u32 WS_Y = 155648;
constexpr u32 WS_O2 = 614400;
constexpr u32 WS_ML1 = 679936;
constexpr u32 WS_ML2 = 745472;
constexpr u32 WS_ACC1 = 778240;
constexpr u32 WS_ACC2 = 1302528;
constexpr u32 WS_EP = 3137536;
constexpr u32 WS_FLAG = 3137792;
constexpr u32 WS_END = 3138048;
constexpr u32 WS_PROF = 16u << 20;
constexpr u32 WS_SRD_BYTES = 0x2000000;

struct Params {
  const u16* x; const u16* K; const u16* V; const u16* wq; const u16* wo;
  const u16* w1; const u16* b1; const u16* w2; const u16* b2;
  u16* out; unsigned char* ws;
};

DI u32 rfl(u32 x) { return __builtin_amdgcn_readfirstlane(x); }

DI void stream_role(const Params& p) {
  const u32 wave = rfl(threadIdx.x >> 6);
  const u32 sidx = blockIdx.x - NCHAIN, g = sidx & 7, split = sidx >> 3;
  const u32 row0 = (g * 8192u + (split * 4 + wave) * WKEYS) * 512u;
  const u32 W = 8192u + wave * 38912u;
  const u32 qoff = WS_Q + g * 1024;
  const u32 accoff = WS_ACC1 + (g * 32 + split) * 1024 + wave * 128;
  const u32 mloff = WS_ML1 + (g * 32 + split) * 128;
  const u32 profoff = rfl(WS_PROF + (64 + sidx * 4 + wave) * 1024);
  asm volatile(
    "s_mov_b64 s[40:41], %0\n\t"
    "s_and_b32 s41, s41, 0xffff\n\t"
    "s_mov_b32 s42, 0x10000000\n\t"
    "s_mov_b32 s43, 0x20000\n\t"
    "s_mov_b64 s[44:45], %1\n\t"
    "s_and_b32 s45, s45, 0xffff\n\t"
    "s_mov_b32 s46, 0x10000000\n\t"
    "s_mov_b32 s47, 0x20000\n\t"
    "s_mov_b64 s[48:49], %2\n\t"
    "s_and_b32 s49, s49, 0xffff\n\t"
    "s_mov_b32 s50, 0x2000000\n\t"
    "s_mov_b32 s51, 0x20000\n\t"
    "s_mov_b32 s52, %3\n\t"
    "s_mov_b32 s54, %4\n\t"
    "s_mov_b32 s66, %5\n\t"
    "s_mov_b32 s67, %6\n\t"
    "s_mov_b32 s68, %7\n\t"
    "s_mov_b32 s72, %9\n\t"
    "s_mov_b32 s78, %8\n\t"
    "s_lshl_b32 s70, %9, 8\n\t"
    "s_add_u32 s70, s70, 45056\n\t"
    "s_lshl_b32 s71, %9, 3\n\t"
    "s_mov_b32 s75, %10\n\t"
    "s_mov_b32 s55, 0\n\t"
    "s_mov_b32 s56, 0\n\t"
    "s_mov_b32 s74, 0\n\t"
    "s_mov_b32 s65, 0\n\t"
    "s_mov_b32 s59, 0x3db8aa3b\n\t"
    "s_mov_b32 s64, 896\n\t"
    "s_mov_b32 s60, 0xaaaaaaaa\n\t"
    "s_mov_b32 s61, 0xaaaaaaaa\n\t"
    "s_mov_b32 s62, 0xcccccccc\n\t"
    "s_mov_b32 s63, 0xcccccccc\n\t"
    "v_mbcnt_lo_u32_b32 v8, -1, 0\n\t"
    "v_mbcnt_hi_u32_b32 v8, -1, v8\n\t"
    "v_lshrrev_b32 v9, 5, v8\n\t"
    "v_lshlrev_b32 v9, 1, v9\n\t"
    "v_and_b32 v10, 7, v8\n\t"
    "v_xor_b32 v9, v9, v10\n\t"
    "v_lshlrev_b32 v9, 4, v9\n\t"
    "v_lshrrev_b32 v10, 3, v8\n\t"
    "v_lshl_add_u32 v0, v10, 9, v9\n\t"
    "v_and_b32 v9, 15, v8\n\t"
    "v_lshlrev_b32 v9, 4, v9\n\t"
    "v_lshrrev_b32 v10, 4, v8\n\t"
    "v_lshl_add_u32 v1, v10, 9, v9\n\t"
    "v_and_b32 v9, 1, v8\n\t"
    "v_lshlrev_b32 v11, 1, v9\n\t"
    "v_xor_b32 v11, v11, v10\n\t"
    "v_lshlrev_b32 v11, 4, v11\n\t"
    "v_lshl_add_u32 v11, v9, 9, v11\n\t"
    "v_bfe_u32 v9, v8, 2, 2\n\t"
    "v_lshl_add_u32 v11, v9, 7, v11\n\t"
    "v_bfe_u32 v9, v8, 1, 1\n\t"
    "v_lshl_add_u32 v11, v9, 12, v11\n\t"
    "v_add_u32 v2, s54, v11\n\t"
    "v_bfe_u32 v9, v8, 3, 1\n\t"
    "v_lshlrev_b32 v11, 4, v10\n\t"
    "v_lshl_add_u32 v11, v9, 9, v11\n\t"
    "v_mov_b32 v3, v11\n\t"
    "v_and_b32 v9, 1, v8\n\t"
    "v_lshlrev_b32 v9, 4, v9\n\t"
    "v_mov_b32 v11, 0xffff\n\t"
    "v_lshlrev_b32 v11, v9, v11\n\t"
    "v_bfe_u32 v9, v8, 1, 2\n\t"
    "v_cmp_eq_u32 vcc, 0, v9\n\t"
    "s_nop 1\n\t"
    "v_cndmask_b32 v4, 0, v11, vcc\n\t"
    "v_cmp_eq_u32 vcc, 1, v9\n\t"
    "s_nop 1\n\t"
    "v_cndmask_b32 v5, 0, v11, vcc\n\t"
    "v_cmp_eq_u32 vcc, 2, v9\n\t"
    "s_nop 1\n\t"
    "v_cndmask_b32 v6, 0, v11, vcc\n\t"
    "v_cmp_eq_u32 vcc, 3, v9\n\t"
    "s_nop 1\n\t"
    "v_cndmask_b32 v7, 0, v11, vcc\n\t"
    "s_mov_b32 s53, s52\n\t"
    "s_add_u32 m0, s54, 0x0\n\t"
    "s_nop 0\n\t"
    "buffer_load_dwordx4 v0, s[40:43], s53 offen offset:0 nt lds\n\t"
    "s_add_u32 m0, m0, s64\n\t"
    "s_nop 0\n\t"
    "buffer_load_dwordx4 v0, s[40:43], s53 offen offset:128 nt lds\n\t"
    "s_add_u32 m0, m0, s64\n\t"
    "s_nop 0\n\t"
    "buffer_load_dwordx4 v0, s[40:43], s53 offen offset:256 nt lds\n\t"
    "s_add_u32 m0, m0, s64\n\t"
    "s_nop 0\n\t"
    "buffer_load_dwordx4 v0, s[40:43], s53 offen offset:384 nt lds\n\t"
    "v_lshlrev_b32 v13, 4, v8\n\t"
    "v_mov_b32 v212, v8\n\t"
    "v_lshrrev_b32 v9, 1, v13\n\t"
    "global_load_dwordx4 v[132:135], v13, %11 offset:0\n\t"
    "global_load_dwordx4 v[136:139], v13, %11 offset:1024\n\t"
    "global_load_dwordx4 v[140:143], v13, %11 offset:2048\n\t"
    "global_load_dwordx2 v[144:145], v9, %11 offset:3072\n\t"
    "global_load_dwordx4 v[146:149], v13, %12 offset:0\n\t"
    "global_load_dwordx4 v[150:153], v13, %12 offset:1024\n\t"
    "global_load_dwordx4 v[154:157], v13, %12 offset:2048\n\t"
    "global_load_dwordx2 v[158:159], v9, %12 offset:3072\n\t"
    "global_load_dwordx4 v[160:163], v13, %13 offset:0\n\t"
    "global_load_dwordx4 v[164:167], v13, %13 offset:1024\n\t"
    "global_load_dwordx4 v[168:171], v13, %13 offset:2048\n\t"
    "global_load_dwordx2 v[172:173], v9, %13 offset:3072\n\t"
    "v_mov_b32 v213, 0\n\t"
    "v_mov_b32 v220, 0\n\t"
    "v_mov_b32 v221, 0\n\t"
    "s_mov_b32 s58, 3137536\n\t"
    "buffer_load_dwordx2 v[214:215], v213, s[48:51], s58 offen sc1\n\t"
    "global_load_ushort v213, v213, %14\n\t"
    "s_mov_b64 s[80:81], %15\n\t"
    "s_and_b32 s81, s81, 0xffff\n\t"
    "s_mov_b32 s82, 0x380000\n\t"
    "s_mov_b32 s83, 0x20000\n\t"
    "s_add_u32 s84, %19, 0\n\t"
    "s_min_u32 s84, s84, 447\n\t"
    "s_lshl_b32 s84, s84, 3\n\t"
    "s_add_u32 s84, s84, %18\n\t"
    "s_lshl_b32 s84, s84, 10\n\t"
    "s_add_u32 m0, s54, 26624\n\t"
    "s_nop 0\n\t"
    "buffer_load_dwordx4 v13, s[80:83], s84 offen lds\n\t"
    "s_add_u32 s84, %19, 120\n\t"
    "s_min_u32 s84, s84, 447\n\t"
    "s_lshl_b32 s84, s84, 3\n\t"
    "s_add_u32 s84, s84, %18\n\t"
    "s_lshl_b32 s84, s84, 10\n\t"
    "s_add_u32 m0, s54, 27648\n\t"
    "s_nop 0\n\t"
    "buffer_load_dwordx4 v13, s[80:83], s84 offen lds\n\t"
    "s_add_u32 s84, %19, 240\n\t"
    "s_min_u32 s84, s84, 447\n\t"
    "s_lshl_b32 s84, s84, 3\n\t"
    "s_add_u32 s84, s84, %18\n\t"
    "s_lshl_b32 s84, s84, 10\n\t"
    "s_add_u32 m0, s54, 28672\n\t"
    "s_nop 0\n\t"
    "buffer_load_dwordx4 v13, s[80:83], s84 offen lds\n\t"
    "s_add_u32 s84, %19, 360\n\t"
    "s_min_u32 s84, s84, 447\n\t"
    "s_lshl_b32 s84, s84, 3\n\t"
    "s_add_u32 s84, s84, %18\n\t"
    "s_lshl_b32 s84, s84, 10\n\t"
    "s_add_u32 m0, s54, 29696\n\t"
    "s_nop 0\n\t"
    "buffer_load_dwordx4 v13, s[80:83], s84 offen lds\n\t"
    "s_add_u32 s53, s52, 0x1000\n\t"
    "s_add_u32 m0, s54, 0x1000\n\t"
    "s_nop 0\n\t"
    "buffer_load_dwordx4 v0, s[40:43], s53 offen offset:0 nt lds\n\t"
    "s_add_u32 m0, m0, s64\n\t"
    "s_nop 0\n\t"
    "buffer_load_dwordx4 v0, s[40:43], s53 offen offset:128 nt lds\n\t"
    "s_add_u32 m0, m0, s64\n\t"
    "s_nop 0\n\t"
    "buffer_load_dwordx4 v0, s[40:43], s53 offen offset:256 nt lds\n\t"
    "s_add_u32 m0, m0, s64\n\t"
    "s_nop 0\n\t"
    "buffer_load_dwordx4 v0, s[40:43], s53 offen offset:384 nt lds\n\t"
    "v_and_b32 v9, 31, v8\n\t"
    "v_lshlrev_b32 v9, 2, v9\n\t"
    "v_lshrrev_b32 v10, 5, v8\n\t"
    "v_lshl_add_u32 v9, v10, 9, v9\n\t"
    "buffer_store_dword v220, v9, s[48:51], s67 offen sc1\n\t"
    "s_add_u32 s58, s67, 0x40000\n\t"
    "buffer_store_dword v220, v9, s[48:51], s58 offen sc1\n\t"
    "v_and_b32 v218, 15, v8\n\t"
    "v_lshlrev_b32 v218, 3, v218\n\t"
    "s_cmp_gt_u32 s72, 2\n\t"
    "s_cbranch_scc1 .Lz1_%=\n\t"
    "s_lshl_b32 s58, s72, 15\n\t"
    "s_add_u32 s58, s58, s68\n\t"
    "s_mov_b64 exec, 0xffff\n\t"
    "buffer_store_dwordx2 v[220:221], v218, s[48:51], s58 offen sc1\n\t"
    "s_mov_b64 exec, -1\n\t"
    ".Lz1_%=:\n\t"
    "s_mov_b32 s90, 0xbdd2d3e8\n\t"
    "v_mov_b32 v210, 0xc0135761\n\t"
    "v_mov_b32 v211, 0xc0135761\n\t"
    "s_mul_i32 s92, %17, 6\n\t"
    "s_mov_b32 s86, 0\n\t"
    "s_mov_b64 s[80:81], %16\n\t"
    "s_and_b32 s81, s81, 0xffff\n\t"
    "s_min_u32 s58, %19, 111\n\t"
    "s_lshl_b32 s58, s58, 12\n\t"
    "s_mul_i32 s84, %18, 458752\n\t"
    "s_add_u32 s84, s84, s58\n\t"
    "s_add_u32 m0, s54, 30720\n\t"
    "s_waitcnt vmcnt(10)\n\t"
    "v_add_co_u32 v214, vcc, 1, v214\n\t"
    "s_nop 1\n\t"
    "v_addc_co_u32 v215, vcc, 0, v215, vcc\n\t"
    "v_lshlrev_b32 v48, 16, v132\n\t"
    "v_and_b32 v49, 0xffff0000, v132\n\t"
    "s_waitcnt vmcnt(7)\n\t"
    "buffer_load_dwordx4 v13, s[80:83], s84 offen offset:0 lds\n\t"
    "v_lshlrev_b32 v50, 16, v133\n\t"
    "v_and_b32 v51, 0xffff0000, v133\n\t"
    "v_lshlrev_b32 v52, 16, v134\n\t"
    "v_and_b32 v53, 0xffff0000, v134\n\t"
    "v_lshlrev_b32 v54, 16, v135\n\t"
    "v_and_b32 v55, 0xffff0000, v135\n\t"
    "v_lshlrev_b32 v56, 16, v136\n\t"
    "v_and_b32 v57, 0xffff0000, v136\n\t"
    "v_lshlrev_b32 v58, 16, v137\n\t"
    "v_and_b32 v59, 0xffff0000, v137\n\t"
    "v_lshlrev_b32 v60, 16, v138\n\t"
    "v_and_b32 v61, 0xffff0000, v138\n\t"
    "v_lshlrev_b32 v62, 16, v139\n\t"
    "v_and_b32 v63, 0xffff0000, v139\n\t"
    "v_lshlrev_b32 v64, 16, v140\n\t"
    "v_and_b32 v65, 0xffff0000, v140\n\t"
    "v_lshlrev_b32 v66, 16, v141\n\t"
    "v_and_b32 v67, 0xffff0000, v141\n\t"
    "v_lshlrev_b32 v68, 16, v142\n\t"
    "v_and_b32 v69, 0xffff0000, v142\n\t"
    "v_lshlrev_b32 v70, 16, v143\n\t"
    "v_and_b32 v71, 0xffff0000, v143\n\t"
    "v_lshlrev_b32 v72, 16, v144\n\t"
    "v_and_b32 v73, 0xffff0000, v144\n\t"
    "v_lshlrev_b32 v74, 16, v145\n\t"
    "v_and_b32 v75, 0xffff0000, v145\n\t"
    "v_lshlrev_b32 v76, 16, v146\n\t"
    "v_and_b32 v77, 0xffff0000, v146\n\t"
    "s_waitcnt vmcnt(7)\n\t"
    "buffer_load_dwordx4 v13, s[80:83], s84 offen offset:1024 lds\n\t"
    "v_lshlrev_b32 v78, 16, v147\n\t"
    "v_and_b32 v79, 0xffff0000, v147\n\t"
    "v_lshlrev_b32 v80, 16, v148\n\t"
    "v_and_b32 v81, 0xffff0000, v148\n\t"
    "v_lshlrev_b32 v82, 16, v149\n\t"
    "v_and_b32 v83, 0xffff0000, v149\n\t"
    "v_lshlrev_b32 v84, 16, v150\n\t"
    "v_and_b32 v85, 0xffff0000, v150\n\t"
    "v_lshlrev_b32 v86, 16, v151\n\t"
    "v_and_b32 v87, 0xffff0000, v151\n\t"
    "v_lshlrev_b32 v88, 16, v152\n\t"
    "v_and_b32 v89, 0xffff0000, v152\n\t"
    "v_lshlrev_b32 v90, 16, v153\n\t"
    "v_and_b32 v91, 0xffff0000, v153\n\t"
    "v_lshlrev_b32 v92, 16, v154\n\t"
    "v_and_b32 v93, 0xffff0000, v154\n\t"
    "v_lshlrev_b32 v94, 16, v155\n\t"
    "v_and_b32 v95, 0xffff0000, v155\n\t"
    "v_lshlrev_b32 v96, 16, v156\n\t"
    "v_and_b32 v97, 0xffff0000, v156\n\t"
    "v_lshlrev_b32 v98, 16, v157\n\t"
    "v_and_b32 v99, 0xffff0000, v157\n\t"
    "v_lshlrev_b32 v100, 16, v158\n\t"
    "v_and_b32 v101, 0xffff0000, v158\n\t"
    "v_lshlrev_b32 v102, 16, v159\n\t"
    "v_and_b32 v103, 0xffff0000, v159\n\t"
    "v_lshlrev_b32 v104, 16, v160\n\t"
    "v_and_b32 v105, 0xffff0000, v160\n\t"
    "s_waitcnt vmcnt(7)\n\t"
    "buffer_load_dwordx4 v13, s[80:83], s84 offen offset:2048 lds\n\t"
    "v_lshlrev_b32 v106, 16, v161\n\t"
    "v_and_b32 v107, 0xffff0000, v161\n\t"
    "v_lshlrev_b32 v108, 16, v162\n\t"
    "v_and_b32 v109, 0xffff0000, v162\n\t"
    "v_lshlrev_b32 v110, 16, v163\n\t"
    "v_and_b32 v111, 0xffff0000, v163\n\t"
    "v_lshlrev_b32 v112, 16, v164\n\t"
    "v_and_b32 v113, 0xffff0000, v164\n\t"
    "v_lshlrev_b32 v114, 16, v165\n\t"
    "v_and_b32 v115, 0xffff0000, v165\n\t"
    "v_lshlrev_b32 v116, 16, v166\n\t"
    "v_and_b32 v117, 0xffff0000, v166\n\t"
    "v_lshlrev_b32 v118, 16, v167\n\t"
    "v_and_b32 v119, 0xffff0000, v167\n\t"
    "v_lshlrev_b32 v120, 16, v168\n\t"
    "v_and_b32 v121, 0xffff0000, v168\n\t"
    "v_lshlrev_b32 v122, 16, v169\n\t"
    "v_and_b32 v123, 0xffff0000, v169\n\t"
    "v_lshlrev_b32 v124, 16, v170\n\t"
    "v_and_b32 v125, 0xffff0000, v170\n\t"
    "v_lshlrev_b32 v126, 16, v171\n\t"
    "v_and_b32 v127, 0xffff0000, v171\n\t"
    "v_lshlrev_b32 v128, 16, v172\n\t"
    "v_and_b32 v129, 0xffff0000, v172\n\t"
    "v_lshlrev_b32 v130, 16, v173\n\t"
    "v_and_b32 v131, 0xffff0000, v173\n\t"
    "s_waitcnt vmcnt(7)\n\t"
    "buffer_load_dwordx4 v13, s[80:83], s84 offen offset:3072 lds\n\t"
    "s_add_u32 s53, s52, 0x2000\n\t"
    "s_add_u32 m0, s54, 0x2000\n\t"
    "s_waitcnt vmcnt(7)\n\t"
    "buffer_load_dwordx4 v0, s[40:43], s53 offen offset:0 nt lds\n\t"
    "s_add_u32 m0, s54, 0x2380\n\t"
    "s_waitcnt vmcnt(7)\n\t"
    "buffer_load_dwordx4 v0, s[40:43], s53 offen offset:128 nt lds\n\t"
    "s_add_u32 m0, s54, 0x2700\n\t"
    "s_waitcnt vmcnt(7)\n\t"
    "buffer_load_dwordx4 v0, s[40:43], s53 offen offset:256 nt lds\n\t"
    "s_call_b64 s[94:95], .Ltab0_%=\n\t"
    "s_add_u32 m0, s54, 0x2a80\n\t"
    "s_waitcnt vmcnt(7)\n\t"
    "buffer_load_dwordx4 v0, s[40:43], s53 offen offset:384 nt lds\n\t"
    "s_add_u32 s53, s52, 0x3000\n\t"
    "s_add_u32 m0, s54, 0x3000\n\t"
    "s_waitcnt vmcnt(7)\n\t"
    "buffer_load_dwordx4 v0, s[40:43], s53 offen offset:0 nt lds\n\t"
    "s_add_u32 m0, s54, 0x3380\n\t"
    "s_waitcnt vmcnt(7)\n\t"
    "buffer_load_dwordx4 v0, s[40:43], s53 offen offset:128 nt lds\n\t"
    "s_call_b64 s[94:95], .Ltab1_%=\n\t"
    "s_add_u32 m0, s54, 0x3700\n\t"
    "s_waitcnt vmcnt(7)\n\t"
    "buffer_load_dwordx4 v0, s[40:43], s53 offen offset:256 nt lds\n\t"
    "s_add_u32 m0, s54, 0x3a80\n\t"
    "s_waitcnt vmcnt(7)\n\t"
    "buffer_load_dwordx4 v0, s[40:43], s53 offen offset:384 nt lds\n\t"
    "s_mov_b32 s58, 3137792\n\t"
    "buffer_load_dwordx2 v[216:217], v218, s[48:51], s58 offen sc1\n\t"
    "s_add_u32 s53, s52, 0x6000\n\t"
    "s_add_u32 m0, s54, 0x4000\n\t"
    "s_waitcnt vmcnt(7)\n\t"
    "buffer_load_dwordx4 v0, s[40:43], s53 offen offset:0 nt lds\n\t"
    "s_call_b64 s[94:95], .Ltab0_%=\n\t"
    "s_add_u32 m0, s54, 0x4380\n\t"
    "s_waitcnt vmcnt(7)\n\t"
    "buffer_load_dwordx4 v0, s[40:43], s53 offen offset:128 nt lds\n\t"
    "s_add_u32 m0, s54, 0x4700\n\t"
    "s_waitcnt vmcnt(7)\n\t"
    "buffer_load_dwordx4 v0, s[40:43], s53 offen offset:256 nt lds\n\t"
    "s_add_u32 m0, s54, 0x4a80\n\t"
    "s_waitcnt vmcnt(7)\n\t"
    "buffer_load_dwordx4 v0, s[40:43], s53 offen offset:384 nt lds\n\t"
    "s_call_b64 s[94:95], .Ltab1_%=\n\t"
    "s_add_u32 s53, s52, 0x7000\n\t"
    "s_add_u32 m0, s54, 0x5000\n\t"
    "s_waitcnt vmcnt(7)\n\t"
    "buffer_load_dwordx4 v0, s[40:43], s53 offen offset:0 nt lds\n\t"
    "s_add_u32 m0, s54, 0x5380\n\t"
    "s_waitcnt vmcnt(7)\n\t"
    "buffer_load_dwordx4 v0, s[40:43], s53 offen offset:128 nt lds\n\t"
    "s_add_u32 m0, s54, 0x5700\n\t"
    "s_waitcnt vmcnt(7)\n\t"
    "buffer_load_dwordx4 v0, s[40:43], s53 offen offset:256 nt lds\n\t"
    "s_call_b64 s[94:95], .Ltab0_%=\n\t"
    "s_add_u32 m0, s54, 0x5a80\n\t"
    "s_waitcnt vmcnt(7)\n\t"
    "buffer_load_dwordx4 v0, s[40:43], s53 offen offset:384 nt lds\n\t"
    "s_add_u32 s53, s52, 0x8000\n\t"
    "s_add_u32 m0, s54, 0x6000\n\t"
    "s_waitcnt vmcnt(7)\n\t"
    "buffer_load_dwordx4 v1, s[40:43], s53 offen offset:0 nt lds\n\t"
    "s_add_u32 m0, s54, 0x6300\n\t"
    "s_waitcnt vmcnt(7)\n\t"
    "buffer_load_dwordx4 v1, s[40:43], s53 offen offset:256 nt lds\n\t"
    "s_call_b64 s[94:95], .Ltab1_%=\n\t"
    "s_waitcnt vmcnt(10)\n\t"
    ".Lfg1_%=:\n\t"
    "v_cmp_ne_u64 vcc, v[216:217], v[214:215]\n\t"
    "s_cbranch_vccz .Lfg2_%=\n\t"
    "s_add_u32 s65, s65, 1\n\t"
    "s_cmp_gt_u32 s65, 0x40000\n\t"
    "s_cbranch_scc1 .Lfg2_%=\n\t"
    "s_sleep 2\n\t"
    "s_mov_b32 s58, 3137792\n\t"
    "buffer_load_dwordx2 v[216:217], v218, s[48:51], s58 offen sc1\n\t"
    "s_waitcnt vmcnt(0)\n\t"
    "s_branch .Lfg1_%=\n\t"
    ".Lfg2_%=:\n\t"
    "s_mov_b64 vcc, -1\n\t"
    "s_branch .Lentry_%=\n\t"
    ".Leven_%=:\n\t"
    "s_add_u32 s53, s52, 0x7000\n\t"
    "s_add_u32 m0, s54, 0x5700\n\t"
    "s_waitcnt vmcnt(7)\n\t"
    "buffer_load_dwordx4 v0, s[40:43], s53 offen offset:256 nt lds\n\t"
    "s_add_u32 m0, s54, 0x5a80\n\t"
    "s_waitcnt vmcnt(7)\n\t"
    "buffer_load_dwordx4 v0, s[40:43], s53 offen offset:384 nt lds\n\t"
    "s_add_u32 s53, s52, 0x8000\n\t"
    "s_add_u32 m0, s54, 0x6000\n\t"
    "s_waitcnt vmcnt(7)\n\t"
    "buffer_load_dwordx4 v1, s[40:43], s53 offen offset:0 nt lds\n\t"
    "s_add_u32 m0, s54, 0x6300\n\t"
    "s_waitcnt vmcnt(7)\n\t"
    "buffer_load_dwordx4 v1, s[40:43], s53 offen offset:256 nt lds\n\t"
    ".Lentry_%=:\n\t"
    "s_waitcnt vmcnt(10)\n\t"
    "ds_read_b128 a[0:3], v2 offset:0\n\t"
    "ds_read_b128 a[4:7], v2 offset:64\n\t"
    "ds_read_b128 a[8:11], v2 offset:1024\n\t"
    "ds_read_b128 a[12:15], v2 offset:1088\n\t"
    "ds_read_b128 a[16:19], v2 offset:2048\n\t"
    "ds_read_b128 a[20:23], v2 offset:2112\n\t"
    "ds_read_b128 a[24:27], v2 offset:3072\n\t"
    "ds_read_b128 a[28:31], v2 offset:3136\n\t"
    "s_waitcnt lgkmcnt(0)\n\t"
    "ds_read_b128 a[32:35], v2 offset:8192\n\t"
    "ds_read_b128 a[36:39], v2 offset:8256\n\t"
    "ds_read_b128 a[40:43], v2 offset:9216\n\t"
    "ds_read_b128 a[44:47], v2 offset:9280\n\t"
    "ds_read_b128 a[48:51], v2 offset:10240\n\t"
    "ds_read_b128 a[52:55], v2 offset:10304\n\t"
    "ds_read_b128 a[56:59], v2 offset:11264\n\t"
    "ds_read_b128 a[60:63], v2 offset:11328\n\t"
    "s_add_u32 s53, s52, 0x4000\n\t"
    "s_add_u32 m0, s54, 0x0\n\t"
    "s_waitcnt vmcnt(7)\n\t"
    "buffer_load_dwordx4 v0, s[40:43], s53 offen offset:0 nt lds\n\t"
    "s_add_u32 m0, s54, 0x380\n\t"
    "s_waitcnt vmcnt(7)\n\t"
    "buffer_load_dwordx4 v0, s[40:43], s53 offen offset:128 nt lds\n\t"
    "s_add_u32 m0, s54, 0x700\n\t"
    "s_waitcnt vmcnt(7)\n\t"
    "buffer_load_dwordx4 v0, s[40:43], s53 offen offset:256 nt lds\n\t"
    "s_cmp_lg_u32 s55, 0\n\t"
    "s_cbranch_scc1 .Ltg6_%=\n\t"
    "s_call_b64 s[94:95], .Ltab0_%=\n\t"
    ".Ltg6_%=:\n\t"
    "s_add_u32 m0, s54, 0xa80\n\t"
    "s_waitcnt vmcnt(7)\n\t"
    "buffer_load_dwordx4 v0, s[40:43], s53 offen offset:384 nt lds\n\t"
    "s_add_u32 s53, s52, 0x5000\n\t"
    "s_add_u32 m0, s54, 0x1000\n\t"
    "s_waitcnt vmcnt(7)\n\t"
    "buffer_load_dwordx4 v0, s[40:43], s53 offen offset:0 nt lds\n\t"
    "s_add_u32 m0, s54, 0x1380\n\t"
    "s_waitcnt vmcnt(7)\n\t"
    "buffer_load_dwordx4 v0, s[40:43], s53 offen offset:128 nt lds\n\t"
    "s_cmp_lg_u32 s55, 0\n\t"
    "s_cbranch_scc1 .Ltg7_%=\n\t"
    "s_call_b64 s[94:95], .Ltab1_%=\n\t"
    ".Ltg7_%=:\n\t"
    "s_add_u32 m0, s54, 0x1700\n\t"
    "s_waitcnt vmcnt(7)\n\t"
    "buffer_load_dwordx4 v0, s[40:43], s53 offen offset:256 nt lds\n\t"
    "s_add_u32 m0, s54, 0x1a80\n\t"
    "s_waitcnt vmcnt(7)\n\t"
    "buffer_load_dwordx4 v0, s[40:43], s53 offen offset:384 nt lds\n\t"
    "s_mov_b32 s53, s52\n\t"
    "s_waitcnt vmcnt(7)\n\t"
    "buffer_load_dwordx4 a[96:99], v1, s[44:47], s53 offen offset:0 nt\n\t"
    "s_cmp_lg_u32 s55, 0\n\t"
    "s_cbranch_scc1 .Ltg8_%=\n\t"
    "s_call_b64 s[94:95], .Ltab0_%=\n\t"
    ".Ltg8_%=:\n\t"
    "s_waitcnt vmcnt(7)\n\t"
    "buffer_load_dwordx4 a[100:103], v1, s[44:47], s53 offen offset:256 nt\n\t"
    "s_waitcnt vmcnt(7)\n\t"
    "buffer_load_dwordx4 a[104:107], v1, s[44:47], s53 offen offset:2048 nt\n\t"
    "s_cmp_lg_u32 s72, 0\n\t"
    "s_cbranch_scc1 .Lql1_%=\n\t"
    "v_mbcnt_lo_u32_b32 v46, -1, 0\n\t"
    "v_mbcnt_hi_u32_b32 v46, -1, v46\n\t"
    "v_lshlrev_b32 v46, 4, v46\n\t"
    "s_lshl_b32 s58, s55, 13\n\t"
    "s_add_u32 s58, s58, s66\n\t"
    "s_mov_b32 m0, 0\n\t"
    "s_waitcnt vmcnt(7)\n\t"
    "buffer_load_dwordx4 v46, s[48:51], s58 offen sc1 lds\n\t"
    ".Lql1_%=:\n\t"
    "s_waitcnt vmcnt(7)\n\t"
    "buffer_load_dwordx4 a[108:111], v1, s[44:47], s53 offen offset:2304 nt\n\t"
    "s_cmp_lg_u32 s55, 0\n\t"
    "s_cbranch_scc1 .Ltg9_%=\n\t"
    "s_call_b64 s[94:95], .Ltab1_%=\n\t"
    ".Ltg9_%=:\n\t"
    "s_add_u32 s53, s52, 0x1000\n\t"
    "s_waitcnt vmcnt(7)\n\t"
    "buffer_load_dwordx4 a[112:115], v1, s[44:47], s53 offen offset:0 nt\n\t"
    "s_waitcnt vmcnt(7)\n\t"
    "buffer_load_dwordx4 a[116:119], v1, s[44:47], s53 offen offset:256 nt\n\t"
    "s_waitcnt vmcnt(7)\n\t"
    "buffer_load_dwordx4 a[120:123], v1, s[44:47], s53 offen offset:2048 nt\n\t"
    "s_cmp_lg_u32 s55, 0\n\t"
    "s_cbranch_scc1 .Ltg10_%=\n\t"
    "s_call_b64 s[94:95], .Ltab0_%=\n\t"
    ".Ltg10_%=:\n\t"
    "s_waitcnt vmcnt(7)\n\t"
    "buffer_load_dwordx4 a[124:127], v1, s[44:47], s53 offen offset:2304 nt\n\t"
    "s_add_u32 s53, s52, 0x2000\n\t"
    "s_waitcnt vmcnt(7)\n\t"
    "buffer_load_dwordx4 a[128:131], v1, s[44:47], s53 offen offset:0 nt\n\t"
    "s_waitcnt vmcnt(7)\n\t"
    "buffer_load_dwordx4 a[132:135], v1, s[44:47], s53 offen offset:256 nt\n\t"
    "s_cmp_lg_u32 s55, 0\n\t"
    "s_cbranch_scc1 .Ltg11_%=\n\t"
    "s_call_b64 s[94:95], .Ltab1_%=\n\t"
    ".Ltg11_%=:\n\t"
    "s_waitcnt vmcnt(7)\n\t"
    "buffer_load_dwordx4 a[136:139], v1, s[44:47], s53 offen offset:2048 nt\n\t"
    "s_waitcnt vmcnt(7)\n\t"
    "buffer_load_dwordx4 a[140:143], v1, s[44:47], s53 offen offset:2304 nt\n\t"
    "s_waitcnt vmcnt(12)\n\t"
    "ds_read_b128 a[64:67], v2 offset:0\n\t"
    "ds_read_b128 a[68:71], v2 offset:64\n\t"
    "ds_read_b128 a[72:75], v2 offset:1024\n\t"
    "ds_read_b128 a[76:79], v2 offset:1088\n\t"
    "ds_read_b128 a[80:83], v2 offset:2048\n\t"
    "ds_read_b128 a[84:87], v2 offset:2112\n\t"
    "ds_read_b128 a[88:91], v2 offset:3072\n\t"
    "ds_read_b128 a[92:95], v2 offset:3136\n\t"
    "s_add_u32 s53, s52, 0x3000\n\t"
    "buffer_load_dwordx4 a[144:147], v1, s[44:47], s53 offen offset:0 nt\n\t"
    "s_cmp_lg_u32 s72, 0\n\t"
    "s_cbranch_scc1 .Lqok2_%=\n\t"
    "v_mbcnt_lo_u32_b32 v46, -1, 0\n\t"
    "v_mbcnt_hi_u32_b32 v46, -1, v46\n\t"
    "v_lshlrev_b32 v46, 4, v46\n\t"
    "s_waitcnt vmcnt(10)\n\t"
    ".Lqchk2_%=:\n\t"
    "ds_read_b128 v[36:39], v46 offset:0\n\t"
    "s_waitcnt lgkmcnt(0)\n\t"
    "v_min3_u32 v36, v36, v37, v38\n\t"
    "v_min_u32 v36, v36, v39\n\t"
    "v_cmp_eq_u32 s[76:77], 0, v36\n\t"
    "s_cmp_eq_u64 s[76:77], 0\n\t"
    "s_cbranch_scc1 .Lqok2_%=\n\t"
    "s_add_u32 s65, s65, 1\n\t"
    "s_cmp_gt_u32 s65, 0x40000\n\t"
    "s_cbranch_scc1 .Lqok2_%=\n\t"
    "s_sleep 1\n\t"
    "s_lshl_b32 s58, s55, 13\n\t"
    "s_add_u32 s58, s58, s66\n\t"
    "s_mov_b32 m0, 0\n\t"
    "s_nop 0\n\t"
    "buffer_load_dwordx4 v46, s[48:51], s58 offen sc1 lds\n\t"
    "s_waitcnt vmcnt(0)\n\t"
    "s_branch .Lqchk2_%=\n\t"
    ".Lqok2_%=:\n\t"
    "v_mbcnt_lo_u32_b32 v46, -1, 0\n\t"
    "v_mbcnt_hi_u32_b32 v46, -1, v46\n\t"
    "v_lshlrev_b32 v46, 4, v46\n\t"
    "v_and_b32 v47, 0xf0, v46\n\t"
    "v_lshrrev_b32 v44, 4, v46\n\t"
    "v_and_b32 v44, 0x30, v44\n\t"
    "v_lshl_add_u32 v44, v47, 2, v44\n\t"
    "v_and_b32 v47, 0x30, v46\n\t"
    "v_add_u32 v46, s54, v44\n\t"
    "v_lshl_add_u32 v47, v47, 2, v3\n\t"
    "s_waitcnt lgkmcnt(0)\n\t"
    "s_barrier\n\t"
    "ds_read_b128 v[36:39], v47 offset:0\n\t"
    "ds_read_b128 v[28:31], v46 offset:24576\n\t"
    "ds_read_b128 v[40:43], v47 offset:256\n\t"
    "ds_read_b128 v[32:35], v46 offset:25600\n\t"
    "s_waitcnt lgkmcnt(2)\n\t"
    "v_mfma_f32_16x16x32_bf16 v[24:27], v[28:31], v[36:39], 0\n\t"
    "ds_read_b128 v[36:39], v3 offset:0\n\t"
    "ds_read_b128 v[28:31], v2 offset:16384\n\t"
    "s_waitcnt lgkmcnt(2)\n\t"
    "v_mfma_f32_16x16x32_bf16 v[24:27], v[32:35], v[40:43], v[24:27]\n\t"
    "ds_read_b128 v[40:43], v3 offset:64\n\t"
    "ds_read_b128 v[32:35], v2 offset:16448\n\t"
    "buffer_load_dwordx4 a[148:151], v1, s[44:47], s53 offen offset:256 nt\n\t"
    "s_waitcnt lgkmcnt(2)\n\t"
    "v_mfma_f32_16x16x32_bf16 v[8:11], a[0:3], v[36:39], 0\n\t"
    "v_mfma_f32_16x16x32_bf16 v[12:15], a[32:35], v[36:39], 0\n\t"
    "v_mfma_f32_16x16x32_bf16 v[16:19], a[64:67], v[36:39], 0\n\t"
    "v_mfma_f32_16x16x32_bf16 v[20:23], v[28:31], v[36:39], 0\n\t"
    "ds_read_b128 v[36:39], v3 offset:128\n\t"
    "ds_read_b128 v[28:31], v2 offset:17408\n\t"
    "s_waitcnt lgkmcnt(2)\n\t"
    "v_mfma_f32_16x16x32_bf16 v[8:11], a[4:7], v[40:43], v[8:11]\n\t"
    "v_mfma_f32_16x16x32_bf16 v[12:15], a[36:39], v[40:43], v[12:15]\n\t"
    "v_mfma_f32_16x16x32_bf16 v[16:19], a[68:71], v[40:43], v[16:19]\n\t"
    "v_mfma_f32_16x16x32_bf16 v[20:23], v[32:35], v[40:43], v[20:23]\n\t"
    "ds_read_b128 v[40:43], v3 offset:192\n\t"
    "ds_read_b128 v[32:35], v2 offset:17472\n\t"
    "s_waitcnt lgkmcnt(2)\n\t"
    "v_mfma_f32_16x16x32_bf16 v[8:11], a[8:11], v[36:39], v[8:11]\n\t"
    "v_mfma_f32_16x16x32_bf16 v[12:15], a[40:43], v[36:39], v[12:15]\n\t"
    "v_mfma_f32_16x16x32_bf16 v[16:19], a[72:75], v[36:39], v[16:19]\n\t"
    "v_mfma_f32_16x16x32_bf16 v[20:23], v[28:31], v[36:39], v[20:23]\n\t"
    "ds_read_b128 v[36:39], v3 offset:256\n\t"
    "ds_read_b128 v[28:31], v2 offset:18432\n\t"
    "v_cndmask_b32 v24, v24, v25, s[60:61]\n\t"
    "v_cndmask_b32 v26, v26, v27, s[60:61]\n\t"
    "v_cndmask_b32 v24, v24, v26, s[62:63]\n\t"
    "s_waitcnt lgkmcnt(2)\n\t"
    "v_mfma_f32_16x16x32_bf16 v[8:11], a[12:15], v[40:43], v[8:11]\n\t"
    "v_mfma_f32_16x16x32_bf16 v[12:15], a[44:47], v[40:43], v[12:15]\n\t"
    "v_mfma_f32_16x16x32_bf16 v[16:19], a[76:79], v[40:43], v[16:19]\n\t"
    "v_mfma_f32_16x16x32_bf16 v[20:23], v[32:35], v[40:43], v[20:23]\n\t"
    "ds_read_b128 v[40:43], v3 offset:320\n\t"
    "ds_read_b128 v[32:35], v2 offset:18496\n\t"
    "v_add_f32_dpp v25, v24, v24 quad_perm:[1,0,3,2] row_mask:0xf bank_mask:0xf\n\t"
    "buffer_load_dwordx4 a[152:155], v1, s[44:47], s53 offen offset:2048 nt\n\t"
    "s_waitcnt lgkmcnt(2)\n\t"
    "v_mfma_f32_16x16x32_bf16 v[8:11], a[16:19], v[36:39], v[8:11]\n\t"
    "v_mfma_f32_16x16x32_bf16 v[12:15], a[48:51], v[36:39], v[12:15]\n\t"
    "v_mfma_f32_16x16x32_bf16 v[16:19], a[80:83], v[36:39], v[16:19]\n\t"
    "v_mfma_f32_16x16x32_bf16 v[20:23], v[28:31], v[36:39], v[20:23]\n\t"
    "ds_read_b128 v[36:39], v3 offset:384\n\t"
    "ds_read_b128 v[28:31], v2 offset:19456\n\t"
    "v_add_f32_dpp v24, v25, v25 quad_perm:[2,3,0,1] row_mask:0xf bank_mask:0xf\n\t"
    "s_waitcnt lgkmcnt(2)\n\t"
    "v_mfma_f32_16x16x32_bf16 v[8:11], a[20:23], v[40:43], v[8:11]\n\t"
    "v_mfma_f32_16x16x32_bf16 v[12:15], a[52:55], v[40:43], v[12:15]\n\t"
    "v_mfma_f32_16x16x32_bf16 v[16:19], a[84:87], v[40:43], v[16:19]\n\t"
    "v_mfma_f32_16x16x32_bf16 v[20:23], v[32:35], v[40:43], v[20:23]\n\t"
    "ds_read_b128 v[40:43], v3 offset:448\n\t"
    "ds_read_b128 v[32:35], v2 offset:19520\n\t"
    "s_waitcnt lgkmcnt(2)\n\t"
    "v_mfma_f32_16x16x32_bf16 v[8:11], a[24:27], v[36:39], v[8:11]\n\t"
    "v_mfma_f32_16x16x32_bf16 v[12:15], a[56:59], v[36:39], v[12:15]\n\t"
    "v_mfma_f32_16x16x32_bf16 v[16:19], a[88:91], v[36:39], v[16:19]\n\t"
    "v_mfma_f32_16x16x32_bf16 v[20:23], v[28:31], v[36:39], v[20:23]\n\t"
    "s_waitcnt lgkmcnt(0)\n\t"
    "v_mfma_f32_16x16x32_bf16 v[8:11], a[28:31], v[40:43], v[8:11]\n\t"
    "v_mfma_f32_16x16x32_bf16 v[12:15], a[60:63], v[40:43], v[12:15]\n\t"
    "v_mfma_f32_16x16x32_bf16 v[16:19], a[92:95], v[40:43], v[16:19]\n\t"
    "v_mfma_f32_16x16x32_bf16 v[20:23], v[32:35], v[40:43], v[20:23]\n\t"
    "buffer_load_dwordx4 a[156:159], v1, s[44:47], s53 offen offset:2304 nt\n\t"
    "s_nop 7\n\t"
    "v_max3_f32 v44, v24, v8, v9\n\t"
    "v_max3_f32 v47, v10, v11, v12\n\t"
    "v_max3_f32 v44, v44, v13, v14\n\t"
    "v_max3_f32 v47, v47, v15, v16\n\t"
    "v_max3_f32 v44, v44, v17, v18\n\t"
    "v_max3_f32 v47, v47, v19, v20\n\t"
    "v_max3_f32 v44, v44, v21, v22\n\t"
    "v_max3_f32 v44, v44, v47, v23\n\t"
    "v_mov_b32 v46, v44\n\t"
    "s_nop 1\n\t"
    "v_permlane16_swap_b32 v44, v46\n\t"
    "v_max_f32 v44, v44, v46\n\t"
    "v_mov_b32 v46, v44\n\t"
    "s_nop 1\n\t"
    "v_permlane32_swap_b32 v44, v46\n\t"
    "v_max_f32 v44, v44, v46\n\t"
    "v_mul_f32 v44, s59, v44\n\t"
    "v_fma_f32 v8, v8, s59, -v44\n\t"
    "v_exp_f32 v8, v8\n\t"
    "v_fma_f32 v9, v9, s59, -v44\n\t"
    "v_exp_f32 v9, v9\n\t"
    "v_mov_b32 v45, v8\n\t"
    "v_cvt_pk_bf16_f32 v8, v8, v8\n\t"
    "v_fma_f32 v10, v10, s59, -v44\n\t"
    "v_exp_f32 v10, v10\n\t"
    "v_add_f32 v45, v45, v9\n\t"
    "v_cvt_pk_bf16_f32 v9, v9, v9\n\t"
    "v_fma_f32 v11, v11, s59, -v44\n\t"
    "v_exp_f32 v11, v11\n\t"
    "v_add_f32 v45, v45, v10\n\t"
    "v_cvt_pk_bf16_f32 v10, v10, v10\n\t"
    "v_fma_f32 v12, v12, s59, -v44\n\t"
    "v_exp_f32 v12, v12\n\t"
    "v_add_f32 v45, v45, v11\n\t"
    "v_cvt_pk_bf16_f32 v11, v11, v11\n\t"
    "s_add_u32 s53, s52, 0x4000\n\t"
    "buffer_load_dwordx4 a[160:163], v1, s[44:47], s53 offen offset:0 nt\n\t"
    "v_fma_f32 v13, v13, s59, -v44\n\t"
    "v_exp_f32 v13, v13\n\t"
    "v_add_f32 v45, v45, v12\n\t"
    "v_cvt_pk_bf16_f32 v12, v12, v12\n\t"
    "v_fma_f32 v14, v14, s59, -v44\n\t"
    "v_exp_f32 v14, v14\n\t"
    "v_add_f32 v45, v45, v13\n\t"
    "v_cvt_pk_bf16_f32 v13, v13, v13\n\t"
    "v_fma_f32 v15, v15, s59, -v44\n\t"
    "v_exp_f32 v15, v15\n\t"
    "v_add_f32 v45, v45, v14\n\t"
    "v_cvt_pk_bf16_f32 v14, v14, v14\n\t"
    "v_fma_f32 v16, v16, s59, -v44\n\t"
    "v_exp_f32 v16, v16\n\t"
    "v_add_f32 v45, v45, v15\n\t"
    "v_cvt_pk_bf16_f32 v15, v15, v15\n\t"
    "v_fma_f32 v17, v17, s59, -v44\n\t"
    "v_exp_f32 v17, v17\n\t"
    "v_add_f32 v45, v45, v16\n\t"
    "v_cvt_pk_bf16_f32 v16, v16, v16\n\t"
    "v_fma_f32 v18, v18, s59, -v44\n\t"
    "v_exp_f32 v18, v18\n\t"
    "v_add_f32 v45, v45, v17\n\t"
    "v_cvt_pk_bf16_f32 v17, v17, v17\n\t"
    "v_fma_f32 v19, v19, s59, -v44\n\t"
    "v_exp_f32 v19, v19\n\t"
    "v_add_f32 v45, v45, v18\n\t"
    "v_cvt_pk_bf16_f32 v18, v18, v18\n\t"
    "v_fma_f32 v20, v20, s59, -v44\n\t"
    "v_exp_f32 v20, v20\n\t"
    "v_add_f32 v45, v45, v19\n\t"
    "v_cvt_pk_bf16_f32 v19, v19, v19\n\t"
    "v_fma_f32 v21, v21, s59, -v44\n\t"
    "v_exp_f32 v21, v21\n\t"
    "v_add_f32 v45, v45, v20\n\t"
    "v_cvt_pk_bf16_f32 v20, v20, v20\n\t"
    "v_fma_f32 v22, v22, s59, -v44\n\t"
    "v_exp_f32 v22, v22\n\t"
    "v_add_f32 v45, v45, v21\n\t"
    "v_cvt_pk_bf16_f32 v21, v21, v21\n\t"
    "buffer_load_dwordx4 a[164:167], v1, s[44:47], s53 offen offset:256 nt\n\t"
    "v_fma_f32 v23, v23, s59, -v44\n\t"
    "v_exp_f32 v23, v23\n\t"
    "v_add_f32 v45, v45, v22\n\t"
    "v_cvt_pk_bf16_f32 v22, v22, v22\n\t"
    "v_fma_f32 v24, v24, s59, -v44\n\t"
    "v_exp_f32 v24, v24\n\t"
    "v_add_f32 v45, v45, v23\n\t"
    "v_cvt_pk_bf16_f32 v23, v23, v23\n\t"
    "s_nop 0\n\t"
    "v_add_f32 v45, v45, v24\n\t"
    "v_cvt_pk_bf16_f32 v24, v24, v24\n\t"
    "s_add_u32 s69, s52, 0x2000000\n\t"
    "v_and_b32 v36, v8, v4\n\t"
    "v_and_b32 v37, v8, v5\n\t"
    "v_and_b32 v38, v8, v6\n\t"
    "v_and_b32 v39, v8, v7\n\t"
    "s_nop 1\n\t"
    "s_waitcnt vmcnt(16)\n\t"
    "v_mfma_f32_16x16x32_bf16 v[28:31], v[36:39], a[96:99], 0\n\t"
    "v_and_b32 v40, v9, v4\n\t"
    "v_and_b32 v41, v9, v5\n\t"
    "v_and_b32 v42, v9, v6\n\t"
    "v_and_b32 v43, v9, v7\n\t"
    "v_mfma_f32_16x16x32_bf16 v[32:35], v[36:39], a[100:103], 0\n\t"
    "s_waitcnt vmcnt(14)\n\t"
    "v_mfma_f32_16x16x32_bf16 v[28:31], v[40:43], a[104:107], v[28:31]\n\t"
    "v_and_b32 v36, v10, v4\n\t"
    "v_and_b32 v37, v10, v5\n\t"
    "v_and_b32 v38, v10, v6\n\t"
    "v_and_b32 v39, v10, v7\n\t"
    "v_mfma_f32_16x16x32_bf16 v[32:35], v[40:43], a[108:111], v[32:35]\n\t"
    "s_waitcnt vmcnt(12)\n\t"
    "v_mfma_f32_16x16x32_bf16 v[28:31], v[36:39], a[112:115], v[28:31]\n\t"
    "v_and_b32 v40, v11, v4\n\t"
    "v_and_b32 v41, v11, v5\n\t"
    "v_and_b32 v42, v11, v6\n\t"
    "v_and_b32 v43, v11, v7\n\t"
    "v_mfma_f32_16x16x32_bf16 v[32:35], v[36:39], a[116:119], v[32:35]\n\t"
    "s_waitcnt vmcnt(10)\n\t"
    "v_mfma_f32_16x16x32_bf16 v[28:31], v[40:43], a[120:123], v[28:31]\n\t"
    "v_and_b32 v36, v12, v4\n\t"
    "v_and_b32 v37, v12, v5\n\t"
    "v_and_b32 v38, v12, v6\n\t"
    "v_and_b32 v39, v12, v7\n\t"
    "v_mfma_f32_16x16x32_bf16 v[32:35], v[40:43], a[124:127], v[32:35]\n\t"
    "s_waitcnt vmcnt(7)\n\t"
    "buffer_load_dwordx4 a[168:171], v1, s[44:47], s53 offen offset:2048 nt\n\t"
    "s_waitcnt vmcnt(9)\n\t"
    "v_mfma_f32_16x16x32_bf16 v[28:31], v[36:39], a[128:131], v[28:31]\n\t"
    "v_and_b32 v40, v13, v4\n\t"
    "v_and_b32 v41, v13, v5\n\t"
    "v_and_b32 v42, v13, v6\n\t"
    "v_and_b32 v43, v13, v7\n\t"
    "v_mfma_f32_16x16x32_bf16 v[32:35], v[36:39], a[132:135], v[32:35]\n\t"
    "s_waitcnt vmcnt(7)\n\t"
    "buffer_load_dwordx4 a[172:175], v1, s[44:47], s53 offen offset:2304 nt\n\t"
    "s_waitcnt vmcnt(8)\n\t"
    "v_mfma_f32_16x16x32_bf16 v[28:31], v[40:43], a[136:139], v[28:31]\n\t"
    "v_and_b32 v36, v14, v4\n\t"
    "v_and_b32 v37, v14, v5\n\t"
    "v_and_b32 v38, v14, v6\n\t"
    "v_and_b32 v39, v14, v7\n\t"
    "v_mfma_f32_16x16x32_bf16 v[32:35], v[40:43], a[140:143], v[32:35]\n\t"
    "s_add_u32 s53, s52, 0x5000\n\t"
    "s_waitcnt vmcnt(7)\n\t"
    "buffer_load_dwordx4 a[176:179], v1, s[44:47], s53 offen offset:0 nt\n\t"
    "s_waitcnt vmcnt(7)\n\t"
    "buffer_load_dwordx4 a[180:183], v1, s[44:47], s53 offen offset:256 nt\n\t"
    "s_waitcnt vmcnt(8)\n\t"
    "v_mfma_f32_16x16x32_bf16 v[28:31], v[36:39], a[144:147], v[28:31]\n\t"
    "v_and_b32 v40, v15, v4\n\t"
    "v_and_b32 v41, v15, v5\n\t"
    "v_and_b32 v42, v15, v6\n\t"
    "v_and_b32 v43, v15, v7\n\t"
    "v_mfma_f32_16x16x32_bf16 v[32:35], v[36:39], a[148:151], v[32:35]\n\t"
    "s_waitcnt vmcnt(7)\n\t"
    "buffer_load_dwordx4 a[184:187], v1, s[44:47], s53 offen offset:2048 nt\n\t"
    "s_waitcnt vmcnt(7)\n\t"
    "buffer_load_dwordx4 a[188:191], v1, s[44:47], s53 offen offset:2304 nt\n\t"
    "s_waitcnt vmcnt(8)\n\t"
    "v_mfma_f32_16x16x32_bf16 v[28:31], v[40:43], a[152:155], v[28:31]\n\t"
    "v_and_b32 v36, v16, v4\n\t"
    "v_and_b32 v37, v16, v5\n\t"
    "v_and_b32 v38, v16, v6\n\t"
    "v_and_b32 v39, v16, v7\n\t"
    "v_mfma_f32_16x16x32_bf16 v[32:35], v[40:43], a[156:159], v[32:35]\n\t"
    "s_add_u32 s53, s52, 0x6000\n\t"
    "s_waitcnt vmcnt(7)\n\t"
    "buffer_load_dwordx4 a[192:195], v1, s[44:47], s53 offen offset:0 nt\n\t"
    "s_waitcnt vmcnt(7)\n\t"
    "buffer_load_dwordx4 a[196:199], v1, s[44:47], s53 offen offset:256 nt\n\t"
    "s_waitcnt vmcnt(8)\n\t"
    "v_mfma_f32_16x16x32_bf16 v[28:31], v[36:39], a[160:163], v[28:31]\n\t"
    "v_and_b32 v40, v17, v4\n\t"
    "v_and_b32 v41, v17, v5\n\t"
    "v_and_b32 v42, v17, v6\n\t"
    "v_and_b32 v43, v17, v7\n\t"
    "v_mfma_f32_16x16x32_bf16 v[32:35], v[36:39], a[164:167], v[32:35]\n\t"
    "s_waitcnt vmcnt(7)\n\t"
    "buffer_load_dwordx4 a[200:203], v1, s[44:47], s53 offen offset:2048 nt\n\t"
    "s_waitcnt vmcnt(7)\n\t"
    "buffer_load_dwordx4 a[204:207], v1, s[44:47], s53 offen offset:2304 nt\n\t"
    "s_waitcnt vmcnt(8)\n\t"
    "v_mfma_f32_16x16x32_bf16 v[28:31], v[40:43], a[168:171], v[28:31]\n\t"
    "v_and_b32 v36, v18, v4\n\t"
    "v_and_b32 v37, v18, v5\n\t"
    "v_and_b32 v38, v18, v6\n\t"
    "v_and_b32 v39, v18, v7\n\t"
    "v_mfma_f32_16x16x32_bf16 v[32:35], v[40:43], a[172:175], v[32:35]\n\t"
    "s_add_u32 s53, s52, 0x7000\n\t"
    "s_waitcnt vmcnt(7)\n\t"
    "buffer_load_dwordx4 a[208:211], v1, s[44:47], s53 offen offset:0 nt\n\t"
    "s_waitcnt vmcnt(7)\n\t"
    "buffer_load_dwordx4 a[212:215], v1, s[44:47], s53 offen offset:256 nt\n\t"
    "s_waitcnt vmcnt(8)\n\t"
    "v_mfma_f32_16x16x32_bf16 v[28:31], v[36:39], a[176:179], v[28:31]\n\t"
    "v_and_b32 v40, v19, v4\n\t"
    "v_and_b32 v41, v19, v5\n\t"
    "v_and_b32 v42, v19, v6\n\t"
    "v_and_b32 v43, v19, v7\n\t"
    "v_mfma_f32_16x16x32_bf16 v[32:35], v[36:39], a[180:183], v[32:35]\n\t"
    "s_waitcnt vmcnt(7)\n\t"
    "buffer_load_dwordx4 a[216:219], v1, s[44:47], s53 offen offset:2048 nt\n\t"
    "s_waitcnt vmcnt(7)\n\t"
    "buffer_load_dwordx4 a[220:223], v1, s[44:47], s53 offen offset:2304 nt\n\t"
    "s_waitcnt vmcnt(8)\n\t"
    "v_mfma_f32_16x16x32_bf16 v[28:31], v[40:43], a[184:187], v[28:31]\n\t"
    "v_and_b32 v36, v20, v4\n\t"
    "v_and_b32 v37, v20, v5\n\t"
    "v_and_b32 v38, v20, v6\n\t"
    "v_and_b32 v39, v20, v7\n\t"
    "v_mfma_f32_16x16x32_bf16 v[32:35], v[40:43], a[188:191], v[32:35]\n\t"
    "s_add_u32 s53, s52, 0x8000\n\t"
    "s_waitcnt vmcnt(7)\n\t"
    "buffer_load_dwordx4 a[224:227], v1, s[44:47], s53 offen offset:0 nt\n\t"
    "s_waitcnt vmcnt(7)\n\t"
    "buffer_load_dwordx4 a[228:231], v1, s[44:47], s53 offen offset:256 nt\n\t"
    "s_waitcnt vmcnt(8)\n\t"
    "v_mfma_f32_16x16x32_bf16 v[28:31], v[36:39], a[192:195], v[28:31]\n\t"
    "v_and_b32 v40, v21, v4\n\t"
    "v_and_b32 v41, v21, v5\n\t"
    "v_and_b32 v42, v21, v6\n\t"
    "v_and_b32 v43, v21, v7\n\t"
    "v_mfma_f32_16x16x32_bf16 v[32:35], v[36:39], a[196:199], v[32:35]\n\t"
    "s_mov_b32 s53, s69\n\t"
    "s_add_u32 m0, s54, 0x0\n\t"
    "s_waitcnt vmcnt(7)\n\t"
    "buffer_load_dwordx4 v0, s[40:43], s53 offen offset:0 nt lds\n\t"
    "s_add_u32 m0, s54, 0x380\n\t"
    "s_waitcnt vmcnt(7)\n\t"
    "buffer_load_dwordx4 v0, s[40:43], s53 offen offset:128 nt lds\n\t"
    "s_waitcnt vmcnt(8)\n\t"
    "v_mfma_f32_16x16x32_bf16 v[28:31], v[40:43], a[200:203], v[28:31]\n\t"
    "v_and_b32 v36, v22, v4\n\t"
    "v_and_b32 v37, v22, v5\n\t"
    "v_and_b32 v38, v22, v6\n\t"
    "v_and_b32 v39, v22, v7\n\t"
    "v_mfma_f32_16x16x32_bf16 v[32:35], v[40:43], a[204:207], v[32:35]\n\t"
    "s_cmp_lg_u32 s72, 0\n\t"
    "s_cbranch_scc1 .Lql3_%=\n\t"
    "v_mbcnt_lo_u32_b32 v46, -1, 0\n\t"
    "v_mbcnt_hi_u32_b32 v46, -1, v46\n\t"
    "v_lshlrev_b32 v46, 4, v46\n\t"
    "s_lshl_b32 s58, s55, 13\n\t"
    "s_add_u32 s58, s58, s66\n\t"
    "s_add_u32 s58, s58, 0xe000\n\t"
    "s_mov_b32 m0, 1024\n\t"
    "s_waitcnt vmcnt(7)\n\t"
    "buffer_load_dwordx4 v46, s[48:51], s58 offen sc1 lds\n\t"
    ".Lql3_%=:\n\t"
    "s_add_u32 m0, s54, 0x700\n\t"
    "s_waitcnt vmcnt(7)\n\t"
    "buffer_load_dwordx4 v0, s[40:43], s53 offen offset:256 nt lds\n\t"
    "s_add_u32 m0, s54, 0xa80\n\t"
    "s_waitcnt vmcnt(7)\n\t"
    "buffer_load_dwordx4 v0, s[40:43], s53 offen offset:384 nt lds\n\t"
    "s_waitcnt vmcnt(8)\n\t"
    "v_mfma_f32_16x16x32_bf16 v[28:31], v[36:39], a[208:211], v[28:31]\n\t"
    "v_and_b32 v40, v23, v4\n\t"
    "v_and_b32 v41, v23, v5\n\t"
    "v_and_b32 v42, v23, v6\n\t"
    "v_and_b32 v43, v23, v7\n\t"
    "v_mfma_f32_16x16x32_bf16 v[32:35], v[36:39], a[212:215], v[32:35]\n\t"
    "s_add_u32 s53, s69, 0x1000\n\t"
    "s_add_u32 m0, s54, 0x1000\n\t"
    "s_waitcnt vmcnt(7)\n\t"
    "buffer_load_dwordx4 v0, s[40:43], s53 offen offset:0 nt lds\n\t"
    "s_add_u32 m0, s54, 0x1380\n\t"
    "s_waitcnt vmcnt(7)\n\t"
    "buffer_load_dwordx4 v0, s[40:43], s53 offen offset:128 nt lds\n\t"
    "s_waitcnt vmcnt(8)\n\t"
    "v_mfma_f32_16x16x32_bf16 v[28:31], v[40:43], a[216:219], v[28:31]\n\t"
    "v_and_b32 v36, v24, v4\n\t"
    "v_and_b32 v37, v24, v5\n\t"
    "v_and_b32 v38, v24, v6\n\t"
    "v_and_b32 v39, v24, v7\n\t"
    "v_mfma_f32_16x16x32_bf16 v[32:35], v[40:43], a[220:223], v[32:35]\n\t"
    "s_add_u32 m0, s54, 0x1700\n\t"
    "s_waitcnt vmcnt(7)\n\t"
    "buffer_load_dwordx4 v0, s[40:43], s53 offen offset:256 nt lds\n\t"
    "s_add_u32 m0, s54, 0x1a80\n\t"
    "s_waitcnt vmcnt(7)\n\t"
    "buffer_load_dwordx4 v0, s[40:43], s53 offen offset:384 nt lds\n\t"
    "s_waitcnt vmcnt(8)\n\t"
    "v_mfma_f32_16x16x32_bf16 v[28:31], v[36:39], a[224:227], v[28:31]\n\t"
    "v_mfma_f32_16x16x32_bf16 v[32:35], v[36:39], a[228:231], v[32:35]\n\t"
    "v_mov_b32 v46, v45\n\t"
    "s_nop 1\n\t"
    "v_permlane16_swap_b32 v45, v46\n\t"
    "v_add_f32 v45, v45, v46\n\t"
    "v_mov_b32 v46, v45\n\t"
    "s_nop 1\n\t"
    "v_permlane32_swap_b32 v45, v46\n\t"
    "v_add_f32 v45, v45, v46\n\t"
    "v_mbcnt_lo_u32_b32 v46, -1, 0\n\t"
    "v_mbcnt_hi_u32_b32 v46, -1, v46\n\t"
    "v_lshlrev_b32 v46, 4, v46\n\t"
    "v_and_b32 v8, 0x2f0, v46\n\t"
    "v_lshrrev_b32 v9, 4, v46\n\t"
    "v_and_b32 v10, 16, v9\n\t"
    "v_lshl_add_u32 v8, v8, 1, v10\n\t"
    "v_add_u32 v8, s54, v8\n\t"
    "ds_write_b128 v8, v[28:31] offset:36864\n\t"
    "ds_write_b128 v8, v[32:35] offset:37376\n\t"
    "v_and_b32 v10, 8, v9\n\t"
    "v_lshl_add_u32 v10, v10, 2, s71\n\t"
    "ds_write_b64 v10, v[44:45] offset:2048\n\t"
    "v_and_b32 v26, 32, v9\n\t"
    "v_and_b32 v47, 31, v9\n\t"
    "v_lshlrev_b32 v24, 3, v47\n\t"
    "v_lshl_add_u32 v24, v26, 5, v24\n\t"
    "v_lshrrev_b32 v46, 1, v24\n\t"
    "v_add_u32 v24, s70, v24\n\t"
    "v_add_u32 v25, 0x13000, v24\n\t"
    "v_lshlrev_b32 v47, 3, v47\n\t"
    "v_lshl_add_u32 v47, v26, 1, v47\n\t"
    "s_waitcnt lgkmcnt(0)\n\t"
    "s_barrier\n\t"
    "ds_read_b128 v[16:19], v26 offset:2048\n\t"
    "ds_read_b128 v[20:23], v26 offset:2064\n\t"
    "ds_read_b64 v[8:9], v24 offset:0\n\t"
    "ds_read_b64 v[10:11], v24 offset:38912\n\t"
    "ds_read_b64 v[12:13], v25 offset:0\n\t"
    "ds_read_b64 v[14:15], v25 offset:38912\n\t"
    "s_and_b32 s58, s55, 1\n\t"
    "s_lshr_b32 s57, s55, 1\n\t"
    "s_add_u32 s57, s57, 1\n\t"
    "s_lshl_b32 s73, s58, 18\n\t"
    "s_lshl_b32 s58, s58, 15\n\t"
    "s_add_u32 s58, s58, s68\n\t"
    "s_add_u32 s73, s73, s67\n\t"
    "s_add_u32 s53, s69, 0x2000\n\t"
    "s_add_u32 m0, s54, 0x2000\n\t"
    "s_nop 0\n\t"
    "buffer_load_dwordx4 v0, s[40:43], s53 offen offset:0 nt lds\n\t"
    "s_waitcnt lgkmcnt(4)\n\t"
    "v_max3_f32 v44, v16, v18, v20\n\t"
    "v_max_f32 v44, v44, v22\n\t"
    "v_bfi_b32 v44, 1, s57, v44\n\t"
    "v_sub_f32 v16, v16, v44\n\t"
    "v_sub_f32 v18, v18, v44\n\t"
    "v_sub_f32 v20, v20, v44\n\t"
    "v_sub_f32 v22, v22, v44\n\t"
    "v_exp_f32 v16, v16\n\t"
    "v_exp_f32 v18, v18\n\t"
    "v_exp_f32 v20, v20\n\t"
    "v_exp_f32 v22, v22\n\t"
    "s_waitcnt lgkmcnt(0)\n\t"
    "s_barrier\n\t"
    "v_pk_mul_f32 v[8:9], v[8:9], v[16:17] op_sel_hi:[1,0]\n\t"
    "v_mul_f32 v45, v17, v16\n\t"
    "v_pk_fma_f32 v[8:9], v[10:11], v[18:19], v[8:9] op_sel_hi:[1,0,1]\n\t"
    "v_fmac_f32 v45, v19, v18\n\t"
    "v_pk_fma_f32 v[8:9], v[12:13], v[20:21], v[8:9] op_sel_hi:[1,0,1]\n\t"
    "v_fmac_f32 v45, v21, v20\n\t"
    "v_pk_fma_f32 v[8:9], v[14:15], v[22:23], v[8:9] op_sel_hi:[1,0,1]\n\t"
    "v_fmac_f32 v45, v23, v22\n\t"
    "v_cvt_pk_bf16_f32 v8, v8, v9\n\t"
    "v_bfi_b32 v45, 1, s57, v45\n\t"
    "v_bfi_b32 v8, 1, s57, v8\n\t"
    "buffer_store_dword v8, v46, s[48:51], s73 offen sc1\n\t"
    "s_cmp_lg_u32 s72, 0\n\t"
    "s_cbranch_scc1 .Lcu4_%=\n\t"
    "s_mov_b32 exec_lo, 0xff\n\t"
    "s_mov_b32 exec_hi, 0xff\n\t"
    "buffer_store_dwordx2 v[44:45], v47, s[48:51], s58 offen sc1\n\t"
    "s_mov_b64 exec, -1\n\t"
    ".Lcu4_%=:\n\t"
    "s_cmp_lg_u32 s72, 0\n\t"
    "s_cbranch_scc1 .Lql5_%=\n\t"
    "v_mbcnt_lo_u32_b32 v46, -1, 0\n\t"
    "v_mbcnt_hi_u32_b32 v46, -1, v46\n\t"
    "v_lshlrev_b32 v46, 4, v46\n\t"
    "s_lshl_b32 s58, s55, 13\n\t"
    "s_add_u32 s58, s58, s66\n\t"
    "s_add_u32 s58, s58, 0xe000\n\t"
    "s_mov_b32 m0, 1024\n\t"
    "s_nop 0\n\t"
    "buffer_load_dwordx4 v46, s[48:51], s58 offen sc1 lds\n\t"
    ".Lql5_%=:\n\t"
    "s_add_u32 m0, s54, 0x2380\n\t"
    "s_nop 0\n\t"
    "buffer_load_dwordx4 v0, s[40:43], s53 offen offset:128 nt lds\n\t"
    "s_add_u32 m0, s54, 0x2700\n\t"
    "s_nop 0\n\t"
    "buffer_load_dwordx4 v0, s[40:43], s53 offen offset:256 nt lds\n\t"
    "s_add_u32 m0, s54, 0x2a80\n\t"
    "s_nop 0\n\t"
    "buffer_load_dwordx4 v0, s[40:43], s53 offen offset:384 nt lds\n\t"
    "s_add_u32 s53, s69, 0x3000\n\t"
    "s_add_u32 m0, s54, 0x3000\n\t"
    "s_nop 0\n\t"
    "buffer_load_dwordx4 v0, s[40:43], s53 offen offset:0 nt lds\n\t"
    "s_cmp_lg_u32 s72, 0\n\t"
    "s_cbranch_scc1 .Lql6_%=\n\t"
    "v_mbcnt_lo_u32_b32 v46, -1, 0\n\t"
    "v_mbcnt_hi_u32_b32 v46, -1, v46\n\t"
    "v_lshlrev_b32 v46, 4, v46\n\t"
    "s_lshl_b32 s58, s55, 13\n\t"
    "s_add_u32 s58, s58, s66\n\t"
    "s_add_u32 s58, s58, 0xe000\n\t"
    "s_mov_b32 m0, 1024\n\t"
    "s_nop 0\n\t"
    "buffer_load_dwordx4 v46, s[48:51], s58 offen sc1 lds\n\t"
    ".Lql6_%=:\n\t"
    "s_add_u32 m0, s54, 0x3380\n\t"
    "s_nop 0\n\t"
    "buffer_load_dwordx4 v0, s[40:43], s53 offen offset:128 nt lds\n\t"
    "s_add_u32 m0, s54, 0x3700\n\t"
    "s_nop 0\n\t"
    "buffer_load_dwordx4 v0, s[40:43], s53 offen offset:256 nt lds\n\t"
    "s_add_u32 m0, s54, 0x3a80\n\t"
    "s_nop 0\n\t"
    "buffer_load_dwordx4 v0, s[40:43], s53 offen offset:384 nt lds\n\t"
    "s_cmp_lg_u32 s55, 0\n\t"
    "s_cbranch_scc1 .Ll2e_%=\n\t"
    "s_add_u32 s53, s69, 0x6000\n\t"
    "s_add_u32 m0, s54, 0x6800\n\t"
    "s_waitcnt vmcnt(7)\n\t"
    "buffer_load_dwordx4 v0, s[40:43], s53 offen offset:0 nt lds\n\t"
    "s_add_u32 m0, s54, 0x6b80\n\t"
    "s_waitcnt vmcnt(7)\n\t"
    "buffer_load_dwordx4 v0, s[40:43], s53 offen offset:128 nt lds\n\t"
    "s_add_u32 m0, s54, 0x6f00\n\t"
    "s_waitcnt vmcnt(7)\n\t"
    "buffer_load_dwordx4 v0, s[40:43], s53 offen offset:256 nt lds\n\t"
    "s_add_u32 m0, s54, 0x7280\n\t"
    "s_waitcnt vmcnt(7)\n\t"
    "buffer_load_dwordx4 v0, s[40:43], s53 offen offset:384 nt lds\n\t"
    "s_add_u32 s53, s69, 0x7000\n\t"
    "s_add_u32 m0, s54, 0x7800\n\t"
    "s_waitcnt vmcnt(7)\n\t"
    "buffer_load_dwordx4 v0, s[40:43], s53 offen offset:0 nt lds\n\t"
    "s_add_u32 m0, s54, 0x7b80\n\t"
    "s_waitcnt vmcnt(7)\n\t"
    "buffer_load_dwordx4 v0, s[40:43], s53 offen offset:128 nt lds\n\t"
    "s_branch .Lskip_%=\n\t"
    ".Ll2e_%=:\n\t"
    "s_cmp_lg_u32 s72, 0\n\t"
    "s_cbranch_scc1 .Lqok7_%=\n\t"
    "v_mbcnt_lo_u32_b32 v46, -1, 0\n\t"
    "v_mbcnt_hi_u32_b32 v46, -1, v46\n\t"
    "v_lshlrev_b32 v46, 4, v46\n\t"
    "s_waitcnt vmcnt(16)\n\t"
    "ds_read_b128 v[36:39], v46 offset:1024\n\t"
    "s_waitcnt lgkmcnt(0)\n\t"
    "v_min3_u32 v36, v36, v37, v38\n\t"
    "v_min_u32 v36, v36, v39\n\t"
    "v_cmp_eq_u32 s[76:77], 0, v36\n\t"
    "s_cmp_eq_u64 s[76:77], 0\n\t"
    "s_cbranch_scc1 .Lqok7_%=\n\t"
    "s_waitcnt vmcnt(8)\n\t"
    "ds_read_b128 v[36:39], v46 offset:1024\n\t"
    "s_waitcnt lgkmcnt(0)\n\t"
    "v_min3_u32 v36, v36, v37, v38\n\t"
    "v_min_u32 v36, v36, v39\n\t"
    "v_cmp_eq_u32 s[76:77], 0, v36\n\t"
    "s_cmp_eq_u64 s[76:77], 0\n\t"
    "s_cbranch_scc1 .Lqok7_%=\n\t"
    "s_waitcnt vmcnt(3)\n\t"
    ".Lqchk7_%=:\n\t"
    "ds_read_b128 v[36:39], v46 offset:1024\n\t"
    "s_waitcnt lgkmcnt(0)\n\t"
    "v_min3_u32 v36, v36, v37, v38\n\t"
    "v_min_u32 v36, v36, v39\n\t"
    "v_cmp_eq_u32 s[76:77], 0, v36\n\t"
    "s_cmp_eq_u64 s[76:77], 0\n\t"
    "s_cbranch_scc1 .Lqok7_%=\n\t"
    "s_add_u32 s65, s65, 1\n\t"
    "s_cmp_gt_u32 s65, 0x40000\n\t"
    "s_cbranch_scc1 .Lqok7_%=\n\t"
    "s_sleep 1\n\t"
    "s_branch .Lqre7_%=\n\t"
    ".Ll2tail_%=:\n\t"
    "s_cmp_lg_u32 s72, 0\n\t"
    "s_cbranch_scc1 .Lqok7_%=\n\t"
    "v_mbcnt_lo_u32_b32 v46, -1, 0\n\t"
    "v_mbcnt_hi_u32_b32 v46, -1, v46\n\t"
    "v_lshlrev_b32 v46, 4, v46\n\t"
    ".Lqre7_%=:\n\t"
    "s_lshl_b32 s58, s55, 13\n\t"
    "s_add_u32 s58, s58, s66\n\t"
    "s_add_u32 s58, s58, 0xe000\n\t"
    "s_mov_b32 m0, 1024\n\t"
    "s_nop 0\n\t"
    "buffer_load_dwordx4 v46, s[48:51], s58 offen sc1 lds\n\t"
    "s_waitcnt vmcnt(0)\n\t"
    "s_branch .Lqchk7_%=\n\t"
    ".Lqok7_%=:\n\t"
    "v_mbcnt_lo_u32_b32 v46, -1, 0\n\t"
    "v_mbcnt_hi_u32_b32 v46, -1, v46\n\t"
    "v_lshlrev_b32 v46, 4, v46\n\t"
    "v_and_b32 v47, 0xf0, v46\n\t"
    "v_lshrrev_b32 v44, 4, v46\n\t"
    "v_and_b32 v44, 0x30, v44\n\t"
    "v_lshl_add_u32 v44, v47, 2, v44\n\t"
    "v_and_b32 v47, 0x30, v46\n\t"
    "v_add_u32 v46, s54, v44\n\t"
    "v_lshl_add_u32 v47, v47, 2, v3\n\t"
    "s_waitcnt lgkmcnt(0)\n\t"
    "s_barrier\n\t"
    "ds_read_b128 v[36:39], v47 offset:1024\n\t"
    "ds_read_b128 v[28:31], v46 offset:34816\n\t"
    "ds_read_b128 v[40:43], v47 offset:1280\n\t"
    "ds_read_b128 v[32:35], v46 offset:35840\n\t"
    "s_waitcnt lgkmcnt(2)\n\t"
    "v_mfma_f32_16x16x32_bf16 v[24:27], v[28:31], v[36:39], 0\n\t"
    "ds_read_b128 v[36:39], v3 offset:1024\n\t"
    "ds_read_b128 v[28:31], v2 offset:26624\n\t"
    "s_waitcnt lgkmcnt(2)\n\t"
    "v_mfma_f32_16x16x32_bf16 v[24:27], v[32:35], v[40:43], v[24:27]\n\t"
    "ds_read_b128 v[40:43], v3 offset:1088\n\t"
    "ds_read_b128 v[32:35], v2 offset:26688\n\t"
    "s_waitcnt lgkmcnt(2)\n\t"
    "v_mfma_f32_16x16x32_bf16 v[8:11], a[232:235], v[36:39], 0\n\t"
    "v_mfma_f32_16x16x32_bf16 v[12:15], v[56:59], v[36:39], 0\n\t"
    "v_mfma_f32_16x16x32_bf16 v[16:19], v[88:91], v[36:39], 0\n\t"
    "v_mfma_f32_16x16x32_bf16 v[20:23], v[28:31], v[36:39], 0\n\t"
    "ds_read_b128 v[36:39], v3 offset:1152\n\t"
    "ds_read_b128 v[28:31], v2 offset:27648\n\t"
    "s_waitcnt lgkmcnt(2)\n\t"
    "v_mfma_f32_16x16x32_bf16 v[8:11], a[236:239], v[40:43], v[8:11]\n\t"
    "v_mfma_f32_16x16x32_bf16 v[12:15], v[60:63], v[40:43], v[12:15]\n\t"
    "v_mfma_f32_16x16x32_bf16 v[16:19], v[92:95], v[40:43], v[16:19]\n\t"
    "v_mfma_f32_16x16x32_bf16 v[20:23], v[32:35], v[40:43], v[20:23]\n\t"
    "ds_read_b128 v[40:43], v3 offset:1216\n\t"
    "ds_read_b128 v[32:35], v2 offset:27712\n\t"
    "s_waitcnt lgkmcnt(2)\n\t"
    "v_mfma_f32_16x16x32_bf16 v[8:11], a[240:243], v[36:39], v[8:11]\n\t"
    "v_mfma_f32_16x16x32_bf16 v[12:15], v[64:67], v[36:39], v[12:15]\n\t"
    "v_mfma_f32_16x16x32_bf16 v[16:19], v[96:99], v[36:39], v[16:19]\n\t"
    "v_mfma_f32_16x16x32_bf16 v[20:23], v[28:31], v[36:39], v[20:23]\n\t"
    "ds_read_b128 v[36:39], v3 offset:1280\n\t"
    "ds_read_b128 v[28:31], v2 offset:28672\n\t"
    "v_cndmask_b32 v24, v24, v25, s[60:61]\n\t"
    "v_cndmask_b32 v26, v26, v27, s[60:61]\n\t"
    "v_cndmask_b32 v24, v24, v26, s[62:63]\n\t"
    "s_waitcnt lgkmcnt(2)\n\t"
    "v_mfma_f32_16x16x32_bf16 v[8:11], a[244:247], v[40:43], v[8:11]\n\t"
    "v_mfma_f32_16x16x32_bf16 v[12:15], v[68:71], v[40:43], v[12:15]\n\t"
    "v_mfma_f32_16x16x32_bf16 v[16:19], v[100:103], v[40:43], v[16:19]\n\t"
    "v_mfma_f32_16x16x32_bf16 v[20:23], v[32:35], v[40:43], v[20:23]\n\t"
    "ds_read_b128 v[40:43], v3 offset:1344\n\t"
    "ds_read_b128 v[32:35], v2 offset:28736\n\t"
    "v_add_f32_dpp v25, v24, v24 quad_perm:[1,0,3,2] row_mask:0xf bank_mask:0xf\n\t"
    "s_waitcnt lgkmcnt(2)\n\t"
    "v_mfma_f32_16x16x32_bf16 v[8:11], a[248:251], v[36:39], v[8:11]\n\t"
    "v_mfma_f32_16x16x32_bf16 v[12:15], v[72:75], v[36:39], v[12:15]\n\t"
    "v_mfma_f32_16x16x32_bf16 v[16:19], v[104:107], v[36:39], v[16:19]\n\t"
    "v_mfma_f32_16x16x32_bf16 v[20:23], v[28:31], v[36:39], v[20:23]\n\t"
    "ds_read_b128 v[36:39], v3 offset:1408\n\t"
    "ds_read_b128 v[28:31], v2 offset:29696\n\t"
    "v_add_f32_dpp v24, v25, v25 quad_perm:[2,3,0,1] row_mask:0xf bank_mask:0xf\n\t"
    "s_waitcnt lgkmcnt(2)\n\t"
    "v_mfma_f32_16x16x32_bf16 v[8:11], a[252:255], v[40:43], v[8:11]\n\t"
    "v_mfma_f32_16x16x32_bf16 v[12:15], v[76:79], v[40:43], v[12:15]\n\t"
    "v_mfma_f32_16x16x32_bf16 v[16:19], v[108:111], v[40:43], v[16:19]\n\t"
    "v_mfma_f32_16x16x32_bf16 v[20:23], v[32:35], v[40:43], v[20:23]\n\t"
    "ds_read_b128 v[40:43], v3 offset:1472\n\t"
    "ds_read_b128 v[32:35], v2 offset:29760\n\t"
    "s_waitcnt lgkmcnt(2)\n\t"
    "v_mfma_f32_16x16x32_bf16 v[8:11], v[48:51], v[36:39], v[8:11]\n\t"
    "v_mfma_f32_16x16x32_bf16 v[12:15], v[80:83], v[36:39], v[12:15]\n\t"
    "v_mfma_f32_16x16x32_bf16 v[16:19], v[112:115], v[36:39], v[16:19]\n\t"
    "v_mfma_f32_16x16x32_bf16 v[20:23], v[28:31], v[36:39], v[20:23]\n\t"
    "s_waitcnt lgkmcnt(0)\n\t"
    "v_mfma_f32_16x16x32_bf16 v[8:11], v[52:55], v[40:43], v[8:11]\n\t"
    "v_mfma_f32_16x16x32_bf16 v[12:15], v[84:87], v[40:43], v[12:15]\n\t"
    "v_mfma_f32_16x16x32_bf16 v[16:19], v[116:119], v[40:43], v[16:19]\n\t"
    "v_mfma_f32_16x16x32_bf16 v[20:23], v[32:35], v[40:43], v[20:23]\n\t"
    "s_nop 7\n\t"
    "s_add_u32 s53, s69, 0x6000\n\t"
    "s_add_u32 m0, s54, 0x6800\n\t"
    "s_nop 0\n\t"
    "buffer_load_dwordx4 v0, s[40:43], s53 offen offset:0 nt lds\n\t"
    "v_max3_f32 v44, v24, v8, v9\n\t"
    "v_max3_f32 v47, v10, v11, v12\n\t"
    "v_max3_f32 v44, v44, v13, v14\n\t"
    "v_max3_f32 v47, v47, v15, v16\n\t"
    "v_max3_f32 v44, v44, v17, v18\n\t"
    "v_max3_f32 v47, v47, v19, v20\n\t"
    "v_max3_f32 v44, v44, v21, v22\n\t"
    "v_max3_f32 v44, v44, v47, v23\n\t"
    "v_mov_b32 v46, v44\n\t"
    "s_nop 1\n\t"
    "v_permlane16_swap_b32 v44, v46\n\t"
    "v_max_f32 v44, v44, v46\n\t"
    "v_mov_b32 v46, v44\n\t"
    "s_nop 1\n\t"
    "v_permlane32_swap_b32 v44, v46\n\t"
    "v_max_f32 v44, v44, v46\n\t"
    "v_mul_f32 v44, s59, v44\n\t"
    "v_fma_f32 v8, v8, s59, -v44\n\t"
    "v_exp_f32 v8, v8\n\t"
    "v_fma_f32 v9, v9, s59, -v44\n\t"
    "v_exp_f32 v9, v9\n\t"
    "v_mov_b32 v45, v8\n\t"
    "v_cvt_pk_bf16_f32 v8, v8, v8\n\t"
    "v_and_b32 v36, v8, v4\n\t"
    "v_and_b32 v37, v8, v5\n\t"
    "v_and_b32 v38, v8, v6\n\t"
    "v_and_b32 v39, v8, v7\n\t"
    "v_fma_f32 v10, v10, s59, -v44\n\t"
    "v_exp_f32 v10, v10\n\t"
    "v_mfma_f32_16x16x32_bf16 v[28:31], v[36:39], v[120:123], 0\n\t"
    "v_add_f32 v45, v45, v9\n\t"
    "v_cvt_pk_bf16_f32 v9, v9, v9\n\t"
    "v_and_b32 v40, v9, v4\n\t"
    "v_and_b32 v41, v9, v5\n\t"
    "v_and_b32 v42, v9, v6\n\t"
    "v_and_b32 v43, v9, v7\n\t"
    "v_mfma_f32_16x16x32_bf16 v[32:35], v[36:39], v[124:127], 0\n\t"
    "v_fma_f32 v11, v11, s59, -v44\n\t"
    "v_exp_f32 v11, v11\n\t"
    "v_mfma_f32_16x16x32_bf16 v[28:31], v[40:43], v[128:131], v[28:31]\n\t"
    "v_add_f32 v45, v45, v10\n\t"
    "v_cvt_pk_bf16_f32 v10, v10, v10\n\t"
    "v_and_b32 v36, v10, v4\n\t"
    "v_and_b32 v37, v10, v5\n\t"
    "v_and_b32 v38, v10, v6\n\t"
    "v_and_b32 v39, v10, v7\n\t"
    "v_mfma_f32_16x16x32_bf16 v[32:35], v[40:43], v[132:135], v[32:35]\n\t"
    "v_fma_f32 v12, v12, s59, -v44\n\t"
    "v_exp_f32 v12, v12\n\t"
    "v_mfma_f32_16x16x32_bf16 v[28:31], v[36:39], v[136:139], v[28:31]\n\t"
    "v_add_f32 v45, v45, v11\n\t"
    "v_cvt_pk_bf16_f32 v11, v11, v11\n\t"
    "v_and_b32 v40, v11, v4\n\t"
    "v_and_b32 v41, v11, v5\n\t"
    "v_and_b32 v42, v11, v6\n\t"
    "v_and_b32 v43, v11, v7\n\t"
    "v_mfma_f32_16x16x32_bf16 v[32:35], v[36:39], v[140:143], v[32:35]\n\t"
    "s_add_u32 m0, s54, 0x6b80\n\t"
    "s_nop 0\n\t"
    "buffer_load_dwordx4 v0, s[40:43], s53 offen offset:128 nt lds\n\t"
    "v_fma_f32 v13, v13, s59, -v44\n\t"
    "v_exp_f32 v13, v13\n\t"
    "v_mfma_f32_16x16x32_bf16 v[28:31], v[40:43], v[144:147], v[28:31]\n\t"
    "v_add_f32 v45, v45, v12\n\t"
    "v_cvt_pk_bf16_f32 v12, v12, v12\n\t"
    "v_and_b32 v36, v12, v4\n\t"
    "v_and_b32 v37, v12, v5\n\t"
    "v_and_b32 v38, v12, v6\n\t"
    "v_and_b32 v39, v12, v7\n\t"
    "v_mfma_f32_16x16x32_bf16 v[32:35], v[40:43], v[148:151], v[32:35]\n\t"
    "v_fma_f32 v14, v14, s59, -v44\n\t"
    "v_exp_f32 v14, v14\n\t"
    "v_mfma_f32_16x16x32_bf16 v[28:31], v[36:39], v[152:155], v[28:31]\n\t"
    "v_add_f32 v45, v45, v13\n\t"
    "v_cvt_pk_bf16_f32 v13, v13, v13\n\t"
    "v_and_b32 v40, v13, v4\n\t"
    "v_and_b32 v41, v13, v5\n\t"
    "v_and_b32 v42, v13, v6\n\t"
    "v_and_b32 v43, v13, v7\n\t"
    "v_mfma_f32_16x16x32_bf16 v[32:35], v[36:39], v[156:159], v[32:35]\n\t"
    "v_fma_f32 v15, v15, s59, -v44\n\t"
    "v_exp_f32 v15, v15\n\t"
    "v_mfma_f32_16x16x32_bf16 v[28:31], v[40:43], v[160:163], v[28:31]\n\t"
    "v_add_f32 v45, v45, v14\n\t"
    "v_cvt_pk_bf16_f32 v14, v14, v14\n\t"
    "v_and_b32 v36, v14, v4\n\t"
    "v_and_b32 v37, v14, v5\n\t"
    "v_and_b32 v38, v14, v6\n\t"
    "v_and_b32 v39, v14, v7\n\t"
    "v_mfma_f32_16x16x32_bf16 v[32:35], v[40:43], v[164:167], v[32:35]\n\t"
    "v_fma_f32 v16, v16, s59, -v44\n\t"
    "v_exp_f32 v16, v16\n\t"
    "v_mfma_f32_16x16x32_bf16 v[28:31], v[36:39], v[168:171], v[28:31]\n\t"
    "v_add_f32 v45, v45, v15\n\t"
    "v_cvt_pk_bf16_f32 v15, v15, v15\n\t"
    "v_and_b32 v40, v15, v4\n\t"
    "v_and_b32 v41, v15, v5\n\t"
    "v_and_b32 v42, v15, v6\n\t"
    "v_and_b32 v43, v15, v7\n\t"
    "v_mfma_f32_16x16x32_bf16 v[32:35], v[36:39], v[172:175], v[32:35]\n\t"
    "v_fma_f32 v17, v17, s59, -v44\n\t"
    "v_exp_f32 v17, v17\n\t"
    "v_mfma_f32_16x16x32_bf16 v[28:31], v[40:43], v[176:179], v[28:31]\n\t"
    "v_add_f32 v45, v45, v16\n\t"
    "v_cvt_pk_bf16_f32 v16, v16, v16\n\t"
    "v_and_b32 v36, v16, v4\n\t"
    "v_and_b32 v37, v16, v5\n\t"
    "v_and_b32 v38, v16, v6\n\t"
    "v_and_b32 v39, v16, v7\n\t"
    "v_mfma_f32_16x16x32_bf16 v[32:35], v[40:43], v[180:183], v[32:35]\n\t"
    "s_add_u32 m0, s54, 0x6f00\n\t"
    "s_nop 0\n\t"
    "buffer_load_dwordx4 v0, s[40:43], s53 offen offset:256 nt lds\n\t"
    "v_fma_f32 v18, v18, s59, -v44\n\t"
    "v_exp_f32 v18, v18\n\t"
    "v_mfma_f32_16x16x32_bf16 v[28:31], v[36:39], v[184:187], v[28:31]\n\t"
    "v_add_f32 v45, v45, v17\n\t"
    "v_cvt_pk_bf16_f32 v17, v17, v17\n\t"
    "v_and_b32 v40, v17, v4\n\t"
    "v_and_b32 v41, v17, v5\n\t"
    "v_and_b32 v42, v17, v6\n\t"
    "v_and_b32 v43, v17, v7\n\t"
    "v_mfma_f32_16x16x32_bf16 v[32:35], v[36:39], v[188:191], v[32:35]\n\t"
    "v_fma_f32 v19, v19, s59, -v44\n\t"
    "v_exp_f32 v19, v19\n\t"
    "v_mfma_f32_16x16x32_bf16 v[28:31], v[40:43], v[192:195], v[28:31]\n\t"
    "v_add_f32 v45, v45, v18\n\t"
    "v_cvt_pk_bf16_f32 v18, v18, v18\n\t"
    "v_and_b32 v36, v18, v4\n\t"
    "v_and_b32 v37, v18, v5\n\t"
    "v_and_b32 v38, v18, v6\n\t"
    "v_and_b32 v39, v18, v7\n\t"
    "v_mfma_f32_16x16x32_bf16 v[32:35], v[40:43], v[196:199], v[32:35]\n\t"
    "v_fma_f32 v20, v20, s59, -v44\n\t"
    "v_exp_f32 v20, v20\n\t"
    "v_mfma_f32_16x16x32_bf16 v[28:31], v[36:39], v[200:203], v[28:31]\n\t"
    "v_add_f32 v45, v45, v19\n\t"
    "v_cvt_pk_bf16_f32 v19, v19, v19\n\t"
    "v_and_b32 v40, v19, v4\n\t"
    "v_and_b32 v41, v19, v5\n\t"
    "v_and_b32 v42, v19, v6\n\t"
    "v_and_b32 v43, v19, v7\n\t"
    "v_mfma_f32_16x16x32_bf16 v[32:35], v[36:39], v[204:207], v[32:35]\n\t"
    "v_fma_f32 v21, v21, s59, -v44\n\t"
    "v_exp_f32 v21, v21\n\t"
    "v_mfma_f32_16x16x32_bf16 v[28:31], v[40:43], v[208:211], v[28:31]\n\t"
    "v_add_f32 v45, v45, v20\n\t"
    "v_cvt_pk_bf16_f32 v20, v20, v20\n\t"
    "v_and_b32 v36, v20, v4\n\t"
    "v_and_b32 v37, v20, v5\n\t"
    "v_and_b32 v38, v20, v6\n\t"
    "v_and_b32 v39, v20, v7\n\t"
    "v_mfma_f32_16x16x32_bf16 v[32:35], v[40:43], v[212:215], v[32:35]\n\t"
    "v_fma_f32 v22, v22, s59, -v44\n\t"
    "v_exp_f32 v22, v22\n\t"
    "v_mfma_f32_16x16x32_bf16 v[28:31], v[36:39], v[216:219], v[28:31]\n\t"
    "v_add_f32 v45, v45, v21\n\t"
    "v_cvt_pk_bf16_f32 v21, v21, v21\n\t"
    "v_and_b32 v40, v21, v4\n\t"
    "v_and_b32 v41, v21, v5\n\t"
    "v_and_b32 v42, v21, v6\n\t"
    "v_and_b32 v43, v21, v7\n\t"
    "v_mfma_f32_16x16x32_bf16 v[32:35], v[36:39], v[220:223], v[32:35]\n\t"
    "s_add_u32 m0, s54, 0x7280\n\t"
    "s_nop 0\n\t"
    "buffer_load_dwordx4 v0, s[40:43], s53 offen offset:384 nt lds\n\t"
    "v_fma_f32 v23, v23, s59, -v44\n\t"
    "v_exp_f32 v23, v23\n\t"
    "v_mfma_f32_16x16x32_bf16 v[28:31], v[40:43], v[224:227], v[28:31]\n\t"
    "v_add_f32 v45, v45, v22\n\t"
    "v_cvt_pk_bf16_f32 v22, v22, v22\n\t"
    "v_and_b32 v36, v22, v4\n\t"
    "v_and_b32 v37, v22, v5\n\t"
    "v_and_b32 v38, v22, v6\n\t"
    "v_and_b32 v39, v22, v7\n\t"
    "v_mfma_f32_16x16x32_bf16 v[32:35], v[40:43], v[228:231], v[32:35]\n\t"
    "v_fma_f32 v24, v24, s59, -v44\n\t"
    "v_exp_f32 v24, v24\n\t"
    "v_mfma_f32_16x16x32_bf16 v[28:31], v[36:39], v[232:235], v[28:31]\n\t"
    "v_add_f32 v45, v45, v23\n\t"
    "v_cvt_pk_bf16_f32 v23, v23, v23\n\t"
    "v_and_b32 v40, v23, v4\n\t"
    "v_and_b32 v41, v23, v5\n\t"
    "v_and_b32 v42, v23, v6\n\t"
    "v_and_b32 v43, v23, v7\n\t"
    "v_mfma_f32_16x16x32_bf16 v[32:35], v[36:39], v[236:239], v[32:35]\n\t"
    "s_nop 1\n\t"
    "v_mfma_f32_16x16x32_bf16 v[28:31], v[40:43], v[240:243], v[28:31]\n\t"
    "v_add_f32 v45, v45, v24\n\t"
    "v_cvt_pk_bf16_f32 v24, v24, v24\n\t"
    "v_and_b32 v36, v24, v4\n\t"
    "v_and_b32 v37, v24, v5\n\t"
    "v_and_b32 v38, v24, v6\n\t"
    "v_and_b32 v39, v24, v7\n\t"
    "v_mfma_f32_16x16x32_bf16 v[32:35], v[40:43], v[244:247], v[32:35]\n\t"
    "s_nop 1\n\t"
    "v_mfma_f32_16x16x32_bf16 v[28:31], v[36:39], v[248:251], v[28:31]\n\t"
    "v_mfma_f32_16x16x32_bf16 v[32:35], v[36:39], v[252:255], v[32:35]\n\t"
    "s_add_u32 s53, s69, 0x7000\n\t"
    "s_add_u32 m0, s54, 0x7800\n\t"
    "s_nop 0\n\t"
    "buffer_load_dwordx4 v0, s[40:43], s53 offen offset:0 nt lds\n\t"
    "v_mov_b32 v46, v45\n\t"
    "s_nop 1\n\t"
    "v_permlane16_swap_b32 v45, v46\n\t"
    "v_add_f32 v45, v45, v46\n\t"
    "v_mov_b32 v46, v45\n\t"
    "s_nop 1\n\t"
    "v_permlane32_swap_b32 v45, v46\n\t"
    "v_add_f32 v45, v45, v46\n\t"
    "v_mbcnt_lo_u32_b32 v46, -1, 0\n\t"
    "v_mbcnt_hi_u32_b32 v46, -1, v46\n\t"
    "v_lshlrev_b32 v46, 4, v46\n\t"
    "v_and_b32 v8, 0x2f0, v46\n\t"
    "v_lshrrev_b32 v9, 4, v46\n\t"
    "v_and_b32 v10, 16, v9\n\t"
    "v_lshl_add_u32 v8, v8, 1, v10\n\t"
    "v_add_u32 v8, s54, v8\n\t"
    "ds_write_b128 v8, v[28:31] offset:36864\n\t"
    "ds_write_b128 v8, v[32:35] offset:37376\n\t"
    "v_and_b32 v10, 8, v9\n\t"
    "v_lshl_add_u32 v10, v10, 2, s71\n\t"
    "ds_write_b64 v10, v[44:45] offset:2048\n\t"
    "v_and_b32 v26, 32, v9\n\t"
    "v_and_b32 v47, 31, v9\n\t"
    "v_lshlrev_b32 v24, 3, v47\n\t"
    "v_lshl_add_u32 v24, v26, 5, v24\n\t"
    "v_lshrrev_b32 v46, 1, v24\n\t"
    "v_add_u32 v24, s70, v24\n\t"
    "v_add_u32 v25, 0x13000, v24\n\t"
    "v_lshlrev_b32 v47, 3, v47\n\t"
    "v_lshl_add_u32 v47, v26, 1, v47\n\t"
    "s_waitcnt lgkmcnt(0)\n\t"
    "s_barrier\n\t"
    "ds_read_b128 v[16:19], v26 offset:2048\n\t"
    "ds_read_b128 v[20:23], v26 offset:2064\n\t"
    "ds_read_b64 v[8:9], v24 offset:0\n\t"
    "ds_read_b64 v[10:11], v24 offset:38912\n\t"
    "ds_read_b64 v[12:13], v25 offset:0\n\t"
    "ds_read_b64 v[14:15], v25 offset:38912\n\t"
    "s_and_b32 s58, s55, 1\n\t"
    "s_lshr_b32 s57, s55, 1\n\t"
    "s_add_u32 s57, s57, 1\n\t"
    "s_lshl_b32 s73, s58, 18\n\t"
    "s_lshl_b32 s58, s58, 15\n\t"
    "s_add_u32 s58, s58, s68\n\t"
    "s_lshl_b32 s79, s55, 18\n\t"
    "s_add_u32 s79, s79, 0x40000\n\t"
    "s_cmp_lt_u32 s55, 8\n\t"
    "s_cselect_b32 s73, s79, s73\n\t"
    "s_cselect_b32 s57, 1, s57\n\t"
    "s_add_u32 s73, s73, s67\n\t"
    "s_add_u32 m0, s54, 0x7b80\n\t"
    "s_nop 0\n\t"
    "buffer_load_dwordx4 v0, s[40:43], s53 offen offset:128 nt lds\n\t"
    "s_waitcnt lgkmcnt(4)\n\t"
    "v_max3_f32 v44, v16, v18, v20\n\t"
    "v_max_f32 v44, v44, v22\n\t"
    "v_bfi_b32 v44, 1, s57, v44\n\t"
    "v_sub_f32 v16, v16, v44\n\t"
    "v_sub_f32 v18, v18, v44\n\t"
    "v_sub_f32 v20, v20, v44\n\t"
    "v_sub_f32 v22, v22, v44\n\t"
    "v_exp_f32 v16, v16\n\t"
    "v_exp_f32 v18, v18\n\t"
    "v_exp_f32 v20, v20\n\t"
    "v_exp_f32 v22, v22\n\t"
    "s_waitcnt lgkmcnt(0)\n\t"
    "s_barrier\n\t"
    "v_pk_mul_f32 v[8:9], v[8:9], v[16:17] op_sel_hi:[1,0]\n\t"
    "v_mul_f32 v45, v17, v16\n\t"
    "v_pk_fma_f32 v[8:9], v[10:11], v[18:19], v[8:9] op_sel_hi:[1,0,1]\n\t"
    "v_fmac_f32 v45, v19, v18\n\t"
    "v_pk_fma_f32 v[8:9], v[12:13], v[20:21], v[8:9] op_sel_hi:[1,0,1]\n\t"
    "v_fmac_f32 v45, v21, v20\n\t"
    "v_pk_fma_f32 v[8:9], v[14:15], v[22:23], v[8:9] op_sel_hi:[1,0,1]\n\t"
    "v_fmac_f32 v45, v23, v22\n\t"
    "v_cvt_pk_bf16_f32 v8, v8, v9\n\t"
    "v_bfi_b32 v45, 1, s57, v45\n\t"
    "v_bfi_b32 v8, 1, s57, v8\n\t"
    "s_sub_u32 s79, s55, 2\n\t"
    "s_cmp_lt_u32 s79, 5\n\t"
    "s_cbranch_scc0 .Lcs8_%=\n\t"
    "s_lshl_b32 s79, s79, 10\n\t"
    "s_lshl_b32 s73, s72, 7\n\t"
    "s_add_u32 s79, s79, s73\n\t"
    "v_add_u32 v9, s79, v46\n\t"
    "ds_write_b32 v9, v8 offset:3072\n\t"
    "s_branch .Lct8_%=\n\t"
    ".Lcs8_%=:\n\t"
    "buffer_store_dword v8, v46, s[48:51], s73 offen sc1\n\t"
    ".Lct8_%=:\n\t"
    "s_cmp_lg_u32 s72, 0\n\t"
    "s_cbranch_scc1 .Lcu8_%=\n\t"
    "s_cmp_lt_u32 s55, 8\n\t"
    "s_cbranch_scc0 .Lcv8_%=\n\t"
    "s_lshl_b32 s79, s55, 4\n\t"
    "v_lshrrev_b32 v9, 2, v26\n\t"
    "v_add_u32 v9, s79, v9\n\t"
    "ds_write_b64 v9, v[44:45] offset:2160\n\t"
    "s_branch .Lcu8_%=\n\t"
    ".Lcv8_%=:\n\t"
    "s_mov_b32 exec_lo, 0xff\n\t"
    "s_mov_b32 exec_hi, 0xff\n\t"
    "buffer_store_dwordx2 v[44:45], v47, s[48:51], s58 offen sc1\n\t"
    "s_mov_b64 exec, -1\n\t"
    ".Lcu8_%=:\n\t"
    "s_cmp_lg_u32 s74, 0\n\t"
    "s_cbranch_scc1 .Lend_%=\n\t"
    ".Lskip_%=:\n\t"
    "s_mov_b32 s52, s69\n\t"
    "s_add_u32 s55, s55, 1\n\t"
    "s_add_u32 s53, s52, 0x7000\n\t"
    "s_add_u32 m0, s54, 0x7f00\n\t"
    "s_waitcnt vmcnt(7)\n\t"
    "buffer_load_dwordx4 v0, s[40:43], s53 offen offset:256 nt lds\n\t"
    "s_add_u32 m0, s54, 0x8280\n\t"
    "s_waitcnt vmcnt(7)\n\t"
    "buffer_load_dwordx4 v0, s[40:43], s53 offen offset:384 nt lds\n\t"
    "s_add_u32 s53, s52, 0x8000\n\t"
    "s_add_u32 m0, s54, 0x8800\n\t"
    "s_waitcnt vmcnt(7)\n\t"
    "buffer_load_dwordx4 v1, s[40:43], s53 offen offset:0 nt lds\n\t"
    "s_add_u32 m0, s54, 0x8b00\n\t"
    "s_waitcnt vmcnt(7)\n\t"
    "buffer_load_dwordx4 v1, s[40:43], s53 offen offset:256 nt lds\n\t"
    "s_waitcnt vmcnt(10)\n\t"
    "ds_read_b128 a[232:235], v2 offset:0\n\t"
    "ds_read_b128 a[236:239], v2 offset:64\n\t"
    "ds_read_b128 a[240:243], v2 offset:1024\n\t"
    "ds_read_b128 a[244:247], v2 offset:1088\n\t"
    "ds_read_b128 a[248:251], v2 offset:2048\n\t"
    "ds_read_b128 a[252:255], v2 offset:2112\n\t"
    "ds_read_b128 v[48:51], v2 offset:3072\n\t"
    "ds_read_b128 v[52:55], v2 offset:3136\n\t"
    "s_waitcnt lgkmcnt(0)\n\t"
    "ds_read_b128 v[56:59], v2 offset:8192\n\t"
    "ds_read_b128 v[60:63], v2 offset:8256\n\t"
    "ds_read_b128 v[64:67], v2 offset:9216\n\t"
    "ds_read_b128 v[68:71], v2 offset:9280\n\t"
    "ds_read_b128 v[72:75], v2 offset:10240\n\t"
    "ds_read_b128 v[76:79], v2 offset:10304\n\t"
    "ds_read_b128 v[80:83], v2 offset:11264\n\t"
    "ds_read_b128 v[84:87], v2 offset:11328\n\t"
    "s_add_u32 s53, s52, 0x4000\n\t"
    "s_add_u32 m0, s54, 0x0\n\t"
    "s_waitcnt vmcnt(7)\n\t"
    "buffer_load_dwordx4 v0, s[40:43], s53 offen offset:0 nt lds\n\t"
    "s_add_u32 m0, s54, 0x380\n\t"
    "s_waitcnt vmcnt(7)\n\t"
    "buffer_load_dwordx4 v0, s[40:43], s53 offen offset:128 nt lds\n\t"
    "s_add_u32 m0, s54, 0x700\n\t"
    "s_waitcnt vmcnt(7)\n\t"
    "buffer_load_dwordx4 v0, s[40:43], s53 offen offset:256 nt lds\n\t"
    "s_add_u32 m0, s54, 0xa80\n\t"
    "s_waitcnt vmcnt(7)\n\t"
    "buffer_load_dwordx4 v0, s[40:43], s53 offen offset:384 nt lds\n\t"
    "s_add_u32 s53, s52, 0x5000\n\t"
    "s_add_u32 m0, s54, 0x1000\n\t"
    "s_waitcnt vmcnt(7)\n\t"
    "buffer_load_dwordx4 v0, s[40:43], s53 offen offset:0 nt lds\n\t"
    "s_add_u32 m0, s54, 0x1380\n\t"
    "s_waitcnt vmcnt(7)\n\t"
    "buffer_load_dwordx4 v0, s[40:43], s53 offen offset:128 nt lds\n\t"
    "s_add_u32 m0, s54, 0x1700\n\t"
    "s_waitcnt vmcnt(7)\n\t"
    "buffer_load_dwordx4 v0, s[40:43], s53 offen offset:256 nt lds\n\t"
    "s_add_u32 m0, s54, 0x1a80\n\t"
    "s_waitcnt vmcnt(7)\n\t"
    "buffer_load_dwordx4 v0, s[40:43], s53 offen offset:384 nt lds\n\t"
    "s_mov_b32 s53, s52\n\t"
    "s_waitcnt vmcnt(7)\n\t"
    "buffer_load_dwordx4 v[120:123], v1, s[44:47], s53 offen offset:0 nt\n\t"
    "s_waitcnt vmcnt(7)\n\t"
    "buffer_load_dwordx4 v[124:127], v1, s[44:47], s53 offen offset:256 nt\n\t"
    "s_waitcnt vmcnt(7)\n\t"
    "buffer_load_dwordx4 v[128:131], v1, s[44:47], s53 offen offset:2048 nt\n\t"
    "s_cmp_lg_u32 s72, 0\n\t"
    "s_cbranch_scc1 .Lql9_%=\n\t"
    "v_mbcnt_lo_u32_b32 v46, -1, 0\n\t"
    "v_mbcnt_hi_u32_b32 v46, -1, v46\n\t"
    "v_lshlrev_b32 v46, 4, v46\n\t"
    "s_lshl_b32 s58, s55, 13\n\t"
    "s_add_u32 s58, s58, s66\n\t"
    "s_mov_b32 m0, 0\n\t"
    "s_waitcnt vmcnt(7)\n\t"
    "buffer_load_dwordx4 v46, s[48:51], s58 offen sc1 lds\n\t"
    ".Lql9_%=:\n\t"
    "s_waitcnt vmcnt(7)\n\t"
    "buffer_load_dwordx4 v[132:135], v1, s[44:47], s53 offen offset:2304 nt\n\t"
    "s_add_u32 s53, s52, 0x1000\n\t"
    "s_waitcnt vmcnt(7)\n\t"
    "buffer_load_dwordx4 v[136:139], v1, s[44:47], s53 offen offset:0 nt\n\t"
    "s_waitcnt vmcnt(7)\n\t"
    "buffer_load_dwordx4 v[140:143], v1, s[44:47], s53 offen offset:256 nt\n\t"
    "s_waitcnt vmcnt(7)\n\t"
    "buffer_load_dwordx4 v[144:147], v1, s[44:47], s53 offen offset:2048 nt\n\t"
    "s_waitcnt vmcnt(7)\n\t"
    "buffer_load_dwordx4 v[148:151], v1, s[44:47], s53 offen offset:2304 nt\n\t"
    "s_add_u32 s53, s52, 0x2000\n\t"
    "s_waitcnt vmcnt(7)\n\t"
    "buffer_load_dwordx4 v[152:155], v1, s[44:47], s53 offen offset:0 nt\n\t"
    "s_waitcnt vmcnt(7)\n\t"
    "buffer_load_dwordx4 v[156:159], v1, s[44:47], s53 offen offset:256 nt\n\t"
    "s_waitcnt vmcnt(7)\n\t"
    "buffer_load_dwordx4 v[160:163], v1, s[44:47], s53 offen offset:2048 nt\n\t"
    "s_waitcnt vmcnt(7)\n\t"
    "buffer_load_dwordx4 v[164:167], v1, s[44:47], s53 offen offset:2304 nt\n\t"
    "s_waitcnt vmcnt(12)\n\t"
    "ds_read_b128 v[88:91], v2 offset:0\n\t"
    "ds_read_b128 v[92:95], v2 offset:64\n\t"
    "ds_read_b128 v[96:99], v2 offset:1024\n\t"
    "ds_read_b128 v[100:103], v2 offset:1088\n\t"
    "ds_read_b128 v[104:107], v2 offset:2048\n\t"
    "ds_read_b128 v[108:111], v2 offset:2112\n\t"
    "ds_read_b128 v[112:115], v2 offset:3072\n\t"
    "ds_read_b128 v[116:119], v2 offset:3136\n\t"
    "s_add_u32 s53, s52, 0x3000\n\t"
    "buffer_load_dwordx4 v[168:171], v1, s[44:47], s53 offen offset:0 nt\n\t"
    "s_cmp_lg_u32 s72, 0\n\t"
    "s_cbranch_scc1 .Lqok10_%=\n\t"
    "v_mbcnt_lo_u32_b32 v46, -1, 0\n\t"
    "v_mbcnt_hi_u32_b32 v46, -1, v46\n\t"
    "v_lshlrev_b32 v46, 4, v46\n\t"
    "s_waitcnt vmcnt(10)\n\t"
    ".Lqchk10_%=:\n\t"
    "ds_read_b128 v[36:39], v46 offset:0\n\t"
    "s_waitcnt lgkmcnt(0)\n\t"
    "v_min3_u32 v36, v36, v37, v38\n\t"
    "v_min_u32 v36, v36, v39\n\t"
    "v_cmp_eq_u32 s[76:77], 0, v36\n\t"
    "s_cmp_eq_u64 s[76:77], 0\n\t"
    "s_cbranch_scc1 .Lqok10_%=\n\t"
    "s_add_u32 s65, s65, 1\n\t"
    "s_cmp_gt_u32 s65, 0x40000\n\t"
    "s_cbranch_scc1 .Lqok10_%=\n\t"
    "s_sleep 1\n\t"
    "s_lshl_b32 s58, s55, 13\n\t"
    "s_add_u32 s58, s58, s66\n\t"
    "s_mov_b32 m0, 0\n\t"
    "s_nop 0\n\t"
    "buffer_load_dwordx4 v46, s[48:51], s58 offen sc1 lds\n\t"
    "s_waitcnt vmcnt(0)\n\t"
    "s_branch .Lqchk10_%=\n\t"
    ".Lqok10_%=:\n\t"
    "v_mbcnt_lo_u32_b32 v46, -1, 0\n\t"
    "v_mbcnt_hi_u32_b32 v46, -1, v46\n\t"
    "v_lshlrev_b32 v46, 4, v46\n\t"
    "v_and_b32 v47, 0xf0, v46\n\t"
    "v_lshrrev_b32 v44, 4, v46\n\t"
    "v_and_b32 v44, 0x30, v44\n\t"
    "v_lshl_add_u32 v44, v47, 2, v44\n\t"
    "v_and_b32 v47, 0x30, v46\n\t"
    "v_add_u32 v46, s54, v44\n\t"
    "v_lshl_add_u32 v47, v47, 2, v3\n\t"
    "s_waitcnt lgkmcnt(0)\n\t"
    "s_barrier\n\t"
    "ds_read_b128 v[36:39], v47 offset:0\n\t"
    "ds_read_b128 v[28:31], v46 offset:34816\n\t"
    "ds_read_b128 v[40:43], v47 offset:256\n\t"
    "ds_read_b128 v[32:35], v46 offset:35840\n\t"
    "s_waitcnt lgkmcnt(2)\n\t"
    "v_mfma_f32_16x16x32_bf16 v[24:27], v[28:31], v[36:39], 0\n\t"
    "ds_read_b128 v[36:39], v3 offset:0\n\t"
    "ds_read_b128 v[28:31], v2 offset:26624\n\t"
    "s_waitcnt lgkmcnt(2)\n\t"
    "v_mfma_f32_16x16x32_bf16 v[24:27], v[32:35], v[40:43], v[24:27]\n\t"
    "ds_read_b128 v[40:43], v3 offset:64\n\t"
    "ds_read_b128 v[32:35], v2 offset:26688\n\t"
    "buffer_load_dwordx4 v[172:175], v1, s[44:47], s53 offen offset:256 nt\n\t"
    "s_waitcnt lgkmcnt(2)\n\t"
    "v_mfma_f32_16x16x32_bf16 v[8:11], a[232:235], v[36:39], 0\n\t"
    "v_mfma_f32_16x16x32_bf16 v[12:15], v[56:59], v[36:39], 0\n\t"
    "v_mfma_f32_16x16x32_bf16 v[16:19], v[88:91], v[36:39], 0\n\t"
    "v_mfma_f32_16x16x32_bf16 v[20:23], v[28:31], v[36:39], 0\n\t"
    "ds_read_b128 v[36:39], v3 offset:128\n\t"
    "ds_read_b128 v[28:31], v2 offset:27648\n\t"
    "s_waitcnt lgkmcnt(2)\n\t"
    "v_mfma_f32_16x16x32_bf16 v[8:11], a[236:239], v[40:43], v[8:11]\n\t"
    "v_mfma_f32_16x16x32_bf16 v[12:15], v[60:63], v[40:43], v[12:15]\n\t"
    "v_mfma_f32_16x16x32_bf16 v[16:19], v[92:95], v[40:43], v[16:19]\n\t"
    "v_mfma_f32_16x16x32_bf16 v[20:23], v[32:35], v[40:43], v[20:23]\n\t"
    "ds_read_b128 v[40:43], v3 offset:192\n\t"
    "ds_read_b128 v[32:35], v2 offset:27712\n\t"
    "s_waitcnt lgkmcnt(2)\n\t"
    "v_mfma_f32_16x16x32_bf16 v[8:11], a[240:243], v[36:39], v[8:11]\n\t"
    "v_mfma_f32_16x16x32_bf16 v[12:15], v[64:67], v[36:39], v[12:15]\n\t"
    "v_mfma_f32_16x16x32_bf16 v[16:19], v[96:99], v[36:39], v[16:19]\n\t"
    "v_mfma_f32_16x16x32_bf16 v[20:23], v[28:31], v[36:39], v[20:23]\n\t"
    "ds_read_b128 v[36:39], v3 offset:256\n\t"
    "ds_read_b128 v[28:31], v2 offset:28672\n\t"
    "v_cndmask_b32 v24, v24, v25, s[60:61]\n\t"
    "v_cndmask_b32 v26, v26, v27, s[60:61]\n\t"
    "v_cndmask_b32 v24, v24, v26, s[62:63]\n\t"
    "s_waitcnt lgkmcnt(2)\n\t"
    "v_mfma_f32_16x16x32_bf16 v[8:11], a[244:247], v[40:43], v[8:11]\n\t"
    "v_mfma_f32_16x16x32_bf16 v[12:15], v[68:71], v[40:43], v[12:15]\n\t"
    "v_mfma_f32_16x16x32_bf16 v[16:19], v[100:103], v[40:43], v[16:19]\n\t"
    "v_mfma_f32_16x16x32_bf16 v[20:23], v[32:35], v[40:43], v[20:23]\n\t"
    "ds_read_b128 v[40:43], v3 offset:320\n\t"
    "ds_read_b128 v[32:35], v2 offset:28736\n\t"
    "v_add_f32_dpp v25, v24, v24 quad_perm:[1,0,3,2] row_mask:0xf bank_mask:0xf\n\t"
    "buffer_load_dwordx4 v[176:179], v1, s[44:47], s53 offen offset:2048 nt\n\t"
    "s_waitcnt lgkmcnt(2)\n\t"
    "v_mfma_f32_16x16x32_bf16 v[8:11], a[248:251], v[36:39], v[8:11]\n\t"
    "v_mfma_f32_16x16x32_bf16 v[12:15], v[72:75], v[36:39], v[12:15]\n\t"
    "v_mfma_f32_16x16x32_bf16 v[16:19], v[104:107], v[36:39], v[16:19]\n\t"
    "v_mfma_f32_16x16x32_bf16 v[20:23], v[28:31], v[36:39], v[20:23]\n\t"
    "ds_read_b128 v[36:39], v3 offset:384\n\t"
    "ds_read_b128 v[28:31], v2 offset:29696\n\t"
    "v_add_f32_dpp v24, v25, v25 quad_perm:[2,3,0,1] row_mask:0xf bank_mask:0xf\n\t"
    "s_waitcnt lgkmcnt(2)\n\t"
    "v_mfma_f32_16x16x32_bf16 v[8:11], a[252:255], v[40:43], v[8:11]\n\t"
    "v_mfma_f32_16x16x32_bf16 v[12:15], v[76:79], v[40:43], v[12:15]\n\t"
    "v_mfma_f32_16x16x32_bf16 v[16:19], v[108:111], v[40:43], v[16:19]\n\t"
    "v_mfma_f32_16x16x32_bf16 v[20:23], v[32:35], v[40:43], v[20:23]\n\t"
    "ds_read_b128 v[40:43], v3 offset:448\n\t"
    "ds_read_b128 v[32:35], v2 offset:29760\n\t"
    "s_waitcnt lgkmcnt(2)\n\t"
    "v_mfma_f32_16x16x32_bf16 v[8:11], v[48:51], v[36:39], v[8:11]\n\t"
    "v_mfma_f32_16x16x32_bf16 v[12:15], v[80:83], v[36:39], v[12:15]\n\t"
    "v_mfma_f32_16x16x32_bf16 v[16:19], v[112:115], v[36:39], v[16:19]\n\t"
    "v_mfma_f32_16x16x32_bf16 v[20:23], v[28:31], v[36:39], v[20:23]\n\t"
    "s_waitcnt lgkmcnt(0)\n\t"
    "v_mfma_f32_16x16x32_bf16 v[8:11], v[52:55], v[40:43], v[8:11]\n\t"
    "v_mfma_f32_16x16x32_bf16 v[12:15], v[84:87], v[40:43], v[12:15]\n\t"
    "v_mfma_f32_16x16x32_bf16 v[16:19], v[116:119], v[40:43], v[16:19]\n\t"
    "v_mfma_f32_16x16x32_bf16 v[20:23], v[32:35], v[40:43], v[20:23]\n\t"
    "buffer_load_dwordx4 v[180:183], v1, s[44:47], s53 offen offset:2304 nt\n\t"
    "s_nop 7\n\t"
    "v_max3_f32 v44, v24, v8, v9\n\t"
    "v_max3_f32 v47, v10, v11, v12\n\t"
    "v_max3_f32 v44, v44, v13, v14\n\t"
    "v_max3_f32 v47, v47, v15, v16\n\t"
    "v_max3_f32 v44, v44, v17, v18\n\t"
    "v_max3_f32 v47, v47, v19, v20\n\t"
    "v_max3_f32 v44, v44, v21, v22\n\t"
    "v_max3_f32 v44, v44, v47, v23\n\t"
    "v_mov_b32 v46, v44\n\t"
    "s_nop 1\n\t"
    "v_permlane16_swap_b32 v44, v46\n\t"
    "v_max_f32 v44, v44, v46\n\t"
    "v_mov_b32 v46, v44\n\t"
    "s_nop 1\n\t"
    "v_permlane32_swap_b32 v44, v46\n\t"
    "v_max_f32 v44, v44, v46\n\t"
    "v_mul_f32 v44, s59, v44\n\t"
    "v_fma_f32 v8, v8, s59, -v44\n\t"
    "v_exp_f32 v8, v8\n\t"
    "v_fma_f32 v9, v9, s59, -v44\n\t"
    "v_exp_f32 v9, v9\n\t"
    "v_mov_b32 v45, v8\n\t"
    "v_cvt_pk_bf16_f32 v8, v8, v8\n\t"
    "v_fma_f32 v10, v10, s59, -v44\n\t"
    "v_exp_f32 v10, v10\n\t"
    "v_add_f32 v45, v45, v9\n\t"
    "v_cvt_pk_bf16_f32 v9, v9, v9\n\t"
    "v_fma_f32 v11, v11, s59, -v44\n\t"
    "v_exp_f32 v11, v11\n\t"
    "v_add_f32 v45, v45, v10\n\t"
    "v_cvt_pk_bf16_f32 v10, v10, v10\n\t"
    "v_fma_f32 v12, v12, s59, -v44\n\t"
    "v_exp_f32 v12, v12\n\t"
    "v_add_f32 v45, v45, v11\n\t"
    "v_cvt_pk_bf16_f32 v11, v11, v11\n\t"
    "s_add_u32 s53, s52, 0x4000\n\t"
    "buffer_load_dwordx4 v[184:187], v1, s[44:47], s53 offen offset:0 nt\n\t"
    "v_fma_f32 v13, v13, s59, -v44\n\t"
    "v_exp_f32 v13, v13\n\t"
    "v_add_f32 v45, v45, v12\n\t"
    "v_cvt_pk_bf16_f32 v12, v12, v12\n\t"
    "v_fma_f32 v14, v14, s59, -v44\n\t"
    "v_exp_f32 v14, v14\n\t"
    "v_add_f32 v45, v45, v13\n\t"
    "v_cvt_pk_bf16_f32 v13, v13, v13\n\t"
    "v_fma_f32 v15, v15, s59, -v44\n\t"
    "v_exp_f32 v15, v15\n\t"
    "v_add_f32 v45, v45, v14\n\t"
    "v_cvt_pk_bf16_f32 v14, v14, v14\n\t"
    "v_fma_f32 v16, v16, s59, -v44\n\t"
    "v_exp_f32 v16, v16\n\t"
    "v_add_f32 v45, v45, v15\n\t"
    "v_cvt_pk_bf16_f32 v15, v15, v15\n\t"
    "v_fma_f32 v17, v17, s59, -v44\n\t"
    "v_exp_f32 v17, v17\n\t"
    "v_add_f32 v45, v45, v16\n\t"
    "v_cvt_pk_bf16_f32 v16, v16, v16\n\t"
    "v_fma_f32 v18, v18, s59, -v44\n\t"
    "v_exp_f32 v18, v18\n\t"
    "v_add_f32 v45, v45, v17\n\t"
    "v_cvt_pk_bf16_f32 v17, v17, v17\n\t"
    "v_fma_f32 v19, v19, s59, -v44\n\t"
    "v_exp_f32 v19, v19\n\t"
    "v_add_f32 v45, v45, v18\n\t"
    "v_cvt_pk_bf16_f32 v18, v18, v18\n\t"
    "v_fma_f32 v20, v20, s59, -v44\n\t"
    "v_exp_f32 v20, v20\n\t"
    "v_add_f32 v45, v45, v19\n\t"
    "v_cvt_pk_bf16_f32 v19, v19, v19\n\t"
    "v_fma_f32 v21, v21, s59, -v44\n\t"
    "v_exp_f32 v21, v21\n\t"
    "v_add_f32 v45, v45, v20\n\t"
    "v_cvt_pk_bf16_f32 v20, v20, v20\n\t"
    "v_fma_f32 v22, v22, s59, -v44\n\t"
    "v_exp_f32 v22, v22\n\t"
    "v_add_f32 v45, v45, v21\n\t"
    "v_cvt_pk_bf16_f32 v21, v21, v21\n\t"
    "buffer_load_dwordx4 v[188:191], v1, s[44:47], s53 offen offset:256 nt\n\t"
    "v_fma_f32 v23, v23, s59, -v44\n\t"
    "v_exp_f32 v23, v23\n\t"
    "v_add_f32 v45, v45, v22\n\t"
    "v_cvt_pk_bf16_f32 v22, v22, v22\n\t"
    "v_fma_f32 v24, v24, s59, -v44\n\t"
    "v_exp_f32 v24, v24\n\t"
    "v_add_f32 v45, v45, v23\n\t"
    "v_cvt_pk_bf16_f32 v23, v23, v23\n\t"
    "s_nop 0\n\t"
    "v_add_f32 v45, v45, v24\n\t"
    "v_cvt_pk_bf16_f32 v24, v24, v24\n\t"
    "s_add_u32 s69, s52, 0x2000000\n\t"
    "v_and_b32 v36, v8, v4\n\t"
    "v_and_b32 v37, v8, v5\n\t"
    "v_and_b32 v38, v8, v6\n\t"
    "v_and_b32 v39, v8, v7\n\t"
    "s_nop 1\n\t"
    "s_waitcnt vmcnt(16)\n\t"
    "v_mfma_f32_16x16x32_bf16 v[28:31], v[36:39], v[120:123], 0\n\t"
    "v_and_b32 v40, v9, v4\n\t"
    "v_and_b32 v41, v9, v5\n\t"
    "v_and_b32 v42, v9, v6\n\t"
    "v_and_b32 v43, v9, v7\n\t"
    "v_mfma_f32_16x16x32_bf16 v[32:35], v[36:39], v[124:127], 0\n\t"
    "s_waitcnt vmcnt(14)\n\t"
    "v_mfma_f32_16x16x32_bf16 v[28:31], v[40:43], v[128:131], v[28:31]\n\t"
    "v_and_b32 v36, v10, v4\n\t"
    "v_and_b32 v37, v10, v5\n\t"
    "v_and_b32 v38, v10, v6\n\t"
    "v_and_b32 v39, v10, v7\n\t"
    "v_mfma_f32_16x16x32_bf16 v[32:35], v[40:43], v[132:135], v[32:35]\n\t"
    "s_waitcnt vmcnt(12)\n\t"
    "v_mfma_f32_16x16x32_bf16 v[28:31], v[36:39], v[136:139], v[28:31]\n\t"
    "v_and_b32 v40, v11, v4\n\t"
    "v_and_b32 v41, v11, v5\n\t"
    "v_and_b32 v42, v11, v6\n\t"
    "v_and_b32 v43, v11, v7\n\t"
    "v_mfma_f32_16x16x32_bf16 v[32:35], v[36:39], v[140:143], v[32:35]\n\t"
    "s_waitcnt vmcnt(10)\n\t"
    "v_mfma_f32_16x16x32_bf16 v[28:31], v[40:43], v[144:147], v[28:31]\n\t"
    "v_and_b32 v36, v12, v4\n\t"
    "v_and_b32 v37, v12, v5\n\t"
    "v_and_b32 v38, v12, v6\n\t"
    "v_and_b32 v39, v12, v7\n\t"
    "v_mfma_f32_16x16x32_bf16 v[32:35], v[40:43], v[148:151], v[32:35]\n\t"
    "s_waitcnt vmcnt(7)\n\t"
    "buffer_load_dwordx4 v[192:195], v1, s[44:47], s53 offen offset:2048 nt\n\t"
    "s_waitcnt vmcnt(9)\n\t"
    "v_mfma_f32_16x16x32_bf16 v[28:31], v[36:39], v[152:155], v[28:31]\n\t"
    "v_and_b32 v40, v13, v4\n\t"
    "v_and_b32 v41, v13, v5\n\t"
    "v_and_b32 v42, v13, v6\n\t"
    "v_and_b32 v43, v13, v7\n\t"
    "v_mfma_f32_16x16x32_bf16 v[32:35], v[36:39], v[156:159], v[32:35]\n\t"
    "s_waitcnt vmcnt(7)\n\t"
    "buffer_load_dwordx4 v[196:199], v1, s[44:47], s53 offen offset:2304 nt\n\t"
    "s_waitcnt vmcnt(8)\n\t"
    "v_mfma_f32_16x16x32_bf16 v[28:31], v[40:43], v[160:163], v[28:31]\n\t"
    "v_and_b32 v36, v14, v4\n\t"
    "v_and_b32 v37, v14, v5\n\t"
    "v_and_b32 v38, v14, v6\n\t"
    "v_and_b32 v39, v14, v7\n\t"
    "v_mfma_f32_16x16x32_bf16 v[32:35], v[40:43], v[164:167], v[32:35]\n\t"
    "s_add_u32 s53, s52, 0x5000\n\t"
    "s_waitcnt vmcnt(7)\n\t"
    "buffer_load_dwordx4 v[200:203], v1, s[44:47], s53 offen offset:0 nt\n\t"
    "s_waitcnt vmcnt(7)\n\t"
    "buffer_load_dwordx4 v[204:207], v1, s[44:47], s53 offen offset:256 nt\n\t"
    "s_waitcnt vmcnt(8)\n\t"
    "v_mfma_f32_16x16x32_bf16 v[28:31], v[36:39], v[168:171], v[28:31]\n\t"
    "v_and_b32 v40, v15, v4\n\t"
    "v_and_b32 v41, v15, v5\n\t"
    "v_and_b32 v42, v15, v6\n\t"
    "v_and_b32 v43, v15, v7\n\t"
    "v_mfma_f32_16x16x32_bf16 v[32:35], v[36:39], v[172:175], v[32:35]\n\t"
    "s_waitcnt vmcnt(7)\n\t"
    "buffer_load_dwordx4 v[208:211], v1, s[44:47], s53 offen offset:2048 nt\n\t"
    "s_waitcnt vmcnt(7)\n\t"
    "buffer_load_dwordx4 v[212:215], v1, s[44:47], s53 offen offset:2304 nt\n\t"
    "s_waitcnt vmcnt(8)\n\t"
    "v_mfma_f32_16x16x32_bf16 v[28:31], v[40:43], v[176:179], v[28:31]\n\t"
    "v_and_b32 v36, v16, v4\n\t"
    "v_and_b32 v37, v16, v5\n\t"
    "v_and_b32 v38, v16, v6\n\t"
    "v_and_b32 v39, v16, v7\n\t"
    "v_mfma_f32_16x16x32_bf16 v[32:35], v[40:43], v[180:183], v[32:35]\n\t"
    "s_add_u32 s53, s52, 0x6000\n\t"
    "s_waitcnt vmcnt(7)\n\t"
    "buffer_load_dwordx4 v[216:219], v1, s[44:47], s53 offen offset:0 nt\n\t"
    "s_waitcnt vmcnt(7)\n\t"
    "buffer_load_dwordx4 v[220:223], v1, s[44:47], s53 offen offset:256 nt\n\t"
    "s_waitcnt vmcnt(8)\n\t"
    "v_mfma_f32_16x16x32_bf16 v[28:31], v[36:39], v[184:187], v[28:31]\n\t"
    "v_and_b32 v40, v17, v4\n\t"
    "v_and_b32 v41, v17, v5\n\t"
    "v_and_b32 v42, v17, v6\n\t"
    "v_and_b32 v43, v17, v7\n\t"
    "v_mfma_f32_16x16x32_bf16 v[32:35], v[36:39], v[188:191], v[32:35]\n\t"
    "s_waitcnt vmcnt(7)\n\t"
    "buffer_load_dwordx4 v[224:227], v1, s[44:47], s53 offen offset:2048 nt\n\t"
    "s_waitcnt vmcnt(7)\n\t"
    "buffer_load_dwordx4 v[228:231], v1, s[44:47], s53 offen offset:2304 nt\n\t"
    "s_waitcnt vmcnt(8)\n\t"
    "v_mfma_f32_16x16x32_bf16 v[28:31], v[40:43], v[192:195], v[28:31]\n\t"
    "v_and_b32 v36, v18, v4\n\t"
    "v_and_b32 v37, v18, v5\n\t"
    "v_and_b32 v38, v18, v6\n\t"
    "v_and_b32 v39, v18, v7\n\t"
    "v_mfma_f32_16x16x32_bf16 v[32:35], v[40:43], v[196:199], v[32:35]\n\t"
    "s_add_u32 s53, s52, 0x7000\n\t"
    "s_waitcnt vmcnt(7)\n\t"
    "buffer_load_dwordx4 v[232:235], v1, s[44:47], s53 offen offset:0 nt\n\t"
    "s_waitcnt vmcnt(7)\n\t"
    "buffer_load_dwordx4 v[236:239], v1, s[44:47], s53 offen offset:256 nt\n\t"
    "s_waitcnt vmcnt(8)\n\t"
    "v_mfma_f32_16x16x32_bf16 v[28:31], v[36:39], v[200:203], v[28:31]\n\t"
    "v_and_b32 v40, v19, v4\n\t"
    "v_and_b32 v41, v19, v5\n\t"
    "v_and_b32 v42, v19, v6\n\t"
    "v_and_b32 v43, v19, v7\n\t"
    "v_mfma_f32_16x16x32_bf16 v[32:35], v[36:39], v[204:207], v[32:35]\n\t"
    "s_waitcnt vmcnt(7)\n\t"
    "buffer_load_dwordx4 v[240:243], v1, s[44:47], s53 offen offset:2048 nt\n\t"
    "s_waitcnt vmcnt(7)\n\t"
    "buffer_load_dwordx4 v[244:247], v1, s[44:47], s53 offen offset:2304 nt\n\t"
    "s_waitcnt vmcnt(8)\n\t"
    "v_mfma_f32_16x16x32_bf16 v[28:31], v[40:43], v[208:211], v[28:31]\n\t"
    "v_and_b32 v36, v20, v4\n\t"
    "v_and_b32 v37, v20, v5\n\t"
    "v_and_b32 v38, v20, v6\n\t"
    "v_and_b32 v39, v20, v7\n\t"
    "v_mfma_f32_16x16x32_bf16 v[32:35], v[40:43], v[212:215], v[32:35]\n\t"
    "s_add_u32 s53, s52, 0x8000\n\t"
    "s_waitcnt vmcnt(7)\n\t"
    "buffer_load_dwordx4 v[248:251], v1, s[44:47], s53 offen offset:0 nt\n\t"
    "s_waitcnt vmcnt(7)\n\t"
    "buffer_load_dwordx4 v[252:255], v1, s[44:47], s53 offen offset:256 nt\n\t"
    "s_cmp_eq_u32 s55, 7\n\t"
    "s_cselect_b32 s42, 0, s42\n\t"
    "s_waitcnt vmcnt(8)\n\t"
    "v_mfma_f32_16x16x32_bf16 v[28:31], v[36:39], v[216:219], v[28:31]\n\t"
    "v_and_b32 v40, v21, v4\n\t"
    "v_and_b32 v41, v21, v5\n\t"
    "v_and_b32 v42, v21, v6\n\t"
    "v_and_b32 v43, v21, v7\n\t"
    "v_mfma_f32_16x16x32_bf16 v[32:35], v[36:39], v[220:223], v[32:35]\n\t"
    "s_mov_b32 s53, s69\n\t"
    "s_add_u32 m0, s54, 0x0\n\t"
    "s_waitcnt vmcnt(7)\n\t"
    "buffer_load_dwordx4 v0, s[40:43], s53 offen offset:0 nt lds\n\t"
    "s_add_u32 m0, s54, 0x380\n\t"
    "s_waitcnt vmcnt(7)\n\t"
    "buffer_load_dwordx4 v0, s[40:43], s53 offen offset:128 nt lds\n\t"
    "s_waitcnt vmcnt(8)\n\t"
    "v_mfma_f32_16x16x32_bf16 v[28:31], v[40:43], v[224:227], v[28:31]\n\t"
    "v_and_b32 v36, v22, v4\n\t"
    "v_and_b32 v37, v22, v5\n\t"
    "v_and_b32 v38, v22, v6\n\t"
    "v_and_b32 v39, v22, v7\n\t"
    "v_mfma_f32_16x16x32_bf16 v[32:35], v[40:43], v[228:231], v[32:35]\n\t"
    "s_cmp_lg_u32 s72, 0\n\t"
    "s_cbranch_scc1 .Lql11_%=\n\t"
    "v_mbcnt_lo_u32_b32 v46, -1, 0\n\t"
    "v_mbcnt_hi_u32_b32 v46, -1, v46\n\t"
    "v_lshlrev_b32 v46, 4, v46\n\t"
    "s_lshl_b32 s58, s55, 13\n\t"
    "s_add_u32 s58, s58, s66\n\t"
    "s_add_u32 s58, s58, 0xe000\n\t"
    "s_mov_b32 m0, 1024\n\t"
    "s_waitcnt vmcnt(7)\n\t"
    "buffer_load_dwordx4 v46, s[48:51], s58 offen sc1 lds\n\t"
    ".Lql11_%=:\n\t"
    "s_add_u32 m0, s54, 0x700\n\t"
    "s_waitcnt vmcnt(7)\n\t"
    "buffer_load_dwordx4 v0, s[40:43], s53 offen offset:256 nt lds\n\t"
    "s_add_u32 m0, s54, 0xa80\n\t"
    "s_waitcnt vmcnt(7)\n\t"
    "buffer_load_dwordx4 v0, s[40:43], s53 offen offset:384 nt lds\n\t"
    "s_waitcnt vmcnt(8)\n\t"
    "v_mfma_f32_16x16x32_bf16 v[28:31], v[36:39], v[232:235], v[28:31]\n\t"
    "v_and_b32 v40, v23, v4\n\t"
    "v_and_b32 v41, v23, v5\n\t"
    "v_and_b32 v42, v23, v6\n\t"
    "v_and_b32 v43, v23, v7\n\t"
    "v_mfma_f32_16x16x32_bf16 v[32:35], v[36:39], v[236:239], v[32:35]\n\t"
    "s_add_u32 s53, s69, 0x1000\n\t"
    "s_add_u32 m0, s54, 0x1000\n\t"
    "s_waitcnt vmcnt(7)\n\t"
    "buffer_load_dwordx4 v0, s[40:43], s53 offen offset:0 nt lds\n\t"
    "s_add_u32 m0, s54, 0x1380\n\t"
    "s_waitcnt vmcnt(7)\n\t"
    "buffer_load_dwordx4 v0, s[40:43], s53 offen offset:128 nt lds\n\t"
    "s_waitcnt vmcnt(8)\n\t"
    "v_mfma_f32_16x16x32_bf16 v[28:31], v[40:43], v[240:243], v[28:31]\n\t"
    "v_and_b32 v36, v24, v4\n\t"
    "v_and_b32 v37, v24, v5\n\t"
    "v_and_b32 v38, v24, v6\n\t"
    "v_and_b32 v39, v24, v7\n\t"
    "v_mfma_f32_16x16x32_bf16 v[32:35], v[40:43], v[244:247], v[32:35]\n\t"
    "s_add_u32 m0, s54, 0x1700\n\t"
    "s_waitcnt vmcnt(7)\n\t"
    "buffer_load_dwordx4 v0, s[40:43], s53 offen offset:256 nt lds\n\t"
    "s_add_u32 m0, s54, 0x1a80\n\t"
    "s_waitcnt vmcnt(7)\n\t"
    "buffer_load_dwordx4 v0, s[40:43], s53 offen offset:384 nt lds\n\t"
    "s_waitcnt vmcnt(8)\n\t"
    "v_mfma_f32_16x16x32_bf16 v[28:31], v[36:39], v[248:251], v[28:31]\n\t"
    "v_mfma_f32_16x16x32_bf16 v[32:35], v[36:39], v[252:255], v[32:35]\n\t"
    "v_mov_b32 v46, v45\n\t"
    "s_nop 1\n\t"
    "v_permlane16_swap_b32 v45, v46\n\t"
    "v_add_f32 v45, v45, v46\n\t"
    "v_mov_b32 v46, v45\n\t"
    "s_nop 1\n\t"
    "v_permlane32_swap_b32 v45, v46\n\t"
    "v_add_f32 v45, v45, v46\n\t"
    "v_mbcnt_lo_u32_b32 v46, -1, 0\n\t"
    "v_mbcnt_hi_u32_b32 v46, -1, v46\n\t"
    "v_lshlrev_b32 v46, 4, v46\n\t"
    "v_and_b32 v8, 0x2f0, v46\n\t"
    "v_lshrrev_b32 v9, 4, v46\n\t"
    "v_and_b32 v10, 16, v9\n\t"
    "v_lshl_add_u32 v8, v8, 1, v10\n\t"
    "v_add_u32 v8, s54, v8\n\t"
    "ds_write_b128 v8, v[28:31] offset:36864\n\t"
    "ds_write_b128 v8, v[32:35] offset:37376\n\t"
    "v_and_b32 v10, 8, v9\n\t"
    "v_lshl_add_u32 v10, v10, 2, s71\n\t"
    "ds_write_b64 v10, v[44:45] offset:2048\n\t"
    "v_and_b32 v26, 32, v9\n\t"
    "v_and_b32 v47, 31, v9\n\t"
    "v_lshlrev_b32 v24, 3, v47\n\t"
    "v_lshl_add_u32 v24, v26, 5, v24\n\t"
    "v_lshrrev_b32 v46, 1, v24\n\t"
    "v_add_u32 v24, s70, v24\n\t"
    "v_add_u32 v25, 0x13000, v24\n\t"
    "v_lshlrev_b32 v47, 3, v47\n\t"
    "v_lshl_add_u32 v47, v26, 1, v47\n\t"
    "s_waitcnt lgkmcnt(0)\n\t"
    "s_barrier\n\t"
    "ds_read_b128 v[16:19], v26 offset:2048\n\t"
    "ds_read_b128 v[20:23], v26 offset:2064\n\t"
    "ds_read_b64 v[8:9], v24 offset:0\n\t"
    "ds_read_b64 v[10:11], v24 offset:38912\n\t"
    "ds_read_b64 v[12:13], v25 offset:0\n\t"
    "ds_read_b64 v[14:15], v25 offset:38912\n\t"
    "s_and_b32 s58, s55, 1\n\t"
    "s_lshr_b32 s57, s55, 1\n\t"
    "s_add_u32 s57, s57, 1\n\t"
    "s_lshl_b32 s73, s58, 18\n\t"
    "s_lshl_b32 s58, s58, 15\n\t"
    "s_add_u32 s58, s58, s68\n\t"
    "s_add_u32 s73, s73, s67\n\t"
    "s_add_u32 s53, s69, 0x2000\n\t"
    "s_add_u32 m0, s54, 0x2000\n\t"
    "s_nop 0\n\t"
    "buffer_load_dwordx4 v0, s[40:43], s53 offen offset:0 nt lds\n\t"
    "s_waitcnt lgkmcnt(4)\n\t"
    "v_max3_f32 v44, v16, v18, v20\n\t"
    "v_max_f32 v44, v44, v22\n\t"
    "v_bfi_b32 v44, 1, s57, v44\n\t"
    "v_sub_f32 v16, v16, v44\n\t"
    "v_sub_f32 v18, v18, v44\n\t"
    "v_sub_f32 v20, v20, v44\n\t"
    "v_sub_f32 v22, v22, v44\n\t"
    "v_exp_f32 v16, v16\n\t"
    "v_exp_f32 v18, v18\n\t"
    "v_exp_f32 v20, v20\n\t"
    "v_exp_f32 v22, v22\n\t"
    "s_waitcnt lgkmcnt(0)\n\t"
    "s_barrier\n\t"
    "v_pk_mul_f32 v[8:9], v[8:9], v[16:17] op_sel_hi:[1,0]\n\t"
    "v_mul_f32 v45, v17, v16\n\t"
    "v_pk_fma_f32 v[8:9], v[10:11], v[18:19], v[8:9] op_sel_hi:[1,0,1]\n\t"
    "v_fmac_f32 v45, v19, v18\n\t"
    "v_pk_fma_f32 v[8:9], v[12:13], v[20:21], v[8:9] op_sel_hi:[1,0,1]\n\t"
    "v_fmac_f32 v45, v21, v20\n\t"
    "v_pk_fma_f32 v[8:9], v[14:15], v[22:23], v[8:9] op_sel_hi:[1,0,1]\n\t"
    "v_fmac_f32 v45, v23, v22\n\t"
    "v_cvt_pk_bf16_f32 v8, v8, v9\n\t"
    "v_bfi_b32 v45, 1, s57, v45\n\t"
    "v_bfi_b32 v8, 1, s57, v8\n\t"
    "buffer_store_dword v8, v46, s[48:51], s73 offen sc1\n\t"
    "s_cmp_lg_u32 s72, 0\n\t"
    "s_cbranch_scc1 .Lcu12_%=\n\t"
    "s_mov_b32 exec_lo, 0xff\n\t"
    "s_mov_b32 exec_hi, 0xff\n\t"
    "buffer_store_dwordx2 v[44:45], v47, s[48:51], s58 offen sc1\n\t"
    "s_mov_b64 exec, -1\n\t"
    ".Lcu12_%=:\n\t"
    "s_cmp_lg_u32 s72, 0\n\t"
    "s_cbranch_scc1 .Lql13_%=\n\t"
    "v_mbcnt_lo_u32_b32 v46, -1, 0\n\t"
    "v_mbcnt_hi_u32_b32 v46, -1, v46\n\t"
    "v_lshlrev_b32 v46, 4, v46\n\t"
    "s_lshl_b32 s58, s55, 13\n\t"
    "s_add_u32 s58, s58, s66\n\t"
    "s_add_u32 s58, s58, 0xe000\n\t"
    "s_mov_b32 m0, 1024\n\t"
    "s_nop 0\n\t"
    "buffer_load_dwordx4 v46, s[48:51], s58 offen sc1 lds\n\t"
    ".Lql13_%=:\n\t"
    "s_add_u32 m0, s54, 0x2380\n\t"
    "s_nop 0\n\t"
    "buffer_load_dwordx4 v0, s[40:43], s53 offen offset:128 nt lds\n\t"
    "s_add_u32 m0, s54, 0x2700\n\t"
    "s_nop 0\n\t"
    "buffer_load_dwordx4 v0, s[40:43], s53 offen offset:256 nt lds\n\t"
    "s_add_u32 m0, s54, 0x2a80\n\t"
    "s_nop 0\n\t"
    "buffer_load_dwordx4 v0, s[40:43], s53 offen offset:384 nt lds\n\t"
    "s_add_u32 s53, s69, 0x3000\n\t"
    "s_add_u32 m0, s54, 0x3000\n\t"
    "s_nop 0\n\t"
    "buffer_load_dwordx4 v0, s[40:43], s53 offen offset:0 nt lds\n\t"
    "s_cmp_lg_u32 s72, 0\n\t"
    "s_cbranch_scc1 .Lql14_%=\n\t"
    "v_mbcnt_lo_u32_b32 v46, -1, 0\n\t"
    "v_mbcnt_hi_u32_b32 v46, -1, v46\n\t"
    "v_lshlrev_b32 v46, 4, v46\n\t"
    "s_lshl_b32 s58, s55, 13\n\t"
    "s_add_u32 s58, s58, s66\n\t"
    "s_add_u32 s58, s58, 0xe000\n\t"
    "s_mov_b32 m0, 1024\n\t"
    "s_nop 0\n\t"
    "buffer_load_dwordx4 v46, s[48:51], s58 offen sc1 lds\n\t"
    ".Lql14_%=:\n\t"
    "s_add_u32 m0, s54, 0x3380\n\t"
    "s_nop 0\n\t"
    "buffer_load_dwordx4 v0, s[40:43], s53 offen offset:128 nt lds\n\t"
    "s_add_u32 m0, s54, 0x3700\n\t"
    "s_nop 0\n\t"
    "buffer_load_dwordx4 v0, s[40:43], s53 offen offset:256 nt lds\n\t"
    "s_add_u32 m0, s54, 0x3a80\n\t"
    "s_nop 0\n\t"
    "buffer_load_dwordx4 v0, s[40:43], s53 offen offset:384 nt lds\n\t"
    "s_cmp_lg_u32 s72, 0\n\t"
    "s_cbranch_scc1 .Lqok15_%=\n\t"
    "v_mbcnt_lo_u32_b32 v46, -1, 0\n\t"
    "v_mbcnt_hi_u32_b32 v46, -1, v46\n\t"
    "v_lshlrev_b32 v46, 4, v46\n\t"
    "s_waitcnt vmcnt(16)\n\t"
    "ds_read_b128 v[36:39], v46 offset:1024\n\t"
    "s_waitcnt lgkmcnt(0)\n\t"
    "v_min3_u32 v36, v36, v37, v38\n\t"
    "v_min_u32 v36, v36, v39\n\t"
    "v_cmp_eq_u32 s[76:77], 0, v36\n\t"
    "s_cmp_eq_u64 s[76:77], 0\n\t"
    "s_cbranch_scc1 .Lqok15_%=\n\t"
    "s_waitcnt vmcnt(8)\n\t"
    "ds_read_b128 v[36:39], v46 offset:1024\n\t"
    "s_waitcnt lgkmcnt(0)\n\t"
    "v_min3_u32 v36, v36, v37, v38\n\t"
    "v_min_u32 v36, v36, v39\n\t"
    "v_cmp_eq_u32 s[76:77], 0, v36\n\t"
    "s_cmp_eq_u64 s[76:77], 0\n\t"
    "s_cbranch_scc1 .Lqok15_%=\n\t"
    "s_waitcnt vmcnt(3)\n\t"
    ".Lqchk15_%=:\n\t"
    "ds_read_b128 v[36:39], v46 offset:1024\n\t"
    "s_waitcnt lgkmcnt(0)\n\t"
    "v_min3_u32 v36, v36, v37, v38\n\t"
    "v_min_u32 v36, v36, v39\n\t"
    "v_cmp_eq_u32 s[76:77], 0, v36\n\t"
    "s_cmp_eq_u64 s[76:77], 0\n\t"
    "s_cbranch_scc1 .Lqok15_%=\n\t"
    "s_add_u32 s65, s65, 1\n\t"
    "s_cmp_gt_u32 s65, 0x40000\n\t"
    "s_cbranch_scc1 .Lqok15_%=\n\t"
    "s_sleep 1\n\t"
    "s_lshl_b32 s58, s55, 13\n\t"
    "s_add_u32 s58, s58, s66\n\t"
    "s_add_u32 s58, s58, 0xe000\n\t"
    "s_mov_b32 m0, 1024\n\t"
    "s_nop 0\n\t"
    "buffer_load_dwordx4 v46, s[48:51], s58 offen sc1 lds\n\t"
    "s_waitcnt vmcnt(0)\n\t"
    "s_branch .Lqchk15_%=\n\t"
    ".Lqok15_%=:\n\t"
    "v_mbcnt_lo_u32_b32 v46, -1, 0\n\t"
    "v_mbcnt_hi_u32_b32 v46, -1, v46\n\t"
    "v_lshlrev_b32 v46, 4, v46\n\t"
    "v_and_b32 v47, 0xf0, v46\n\t"
    "v_lshrrev_b32 v44, 4, v46\n\t"
    "v_and_b32 v44, 0x30, v44\n\t"
    "v_lshl_add_u32 v44, v47, 2, v44\n\t"
    "v_and_b32 v47, 0x30, v46\n\t"
    "v_add_u32 v46, s54, v44\n\t"
    "v_lshl_add_u32 v47, v47, 2, v3\n\t"
    "s_waitcnt lgkmcnt(0)\n\t"
    "s_barrier\n\t"
    "ds_read_b128 v[36:39], v47 offset:1024\n\t"
    "ds_read_b128 v[28:31], v46 offset:24576\n\t"
    "ds_read_b128 v[40:43], v47 offset:1280\n\t"
    "ds_read_b128 v[32:35], v46 offset:25600\n\t"
    "s_waitcnt lgkmcnt(2)\n\t"
    "v_mfma_f32_16x16x32_bf16 v[24:27], v[28:31], v[36:39], 0\n\t"
    "ds_read_b128 v[36:39], v3 offset:1024\n\t"
    "ds_read_b128 v[28:31], v2 offset:16384\n\t"
    "s_waitcnt lgkmcnt(2)\n\t"
    "v_mfma_f32_16x16x32_bf16 v[24:27], v[32:35], v[40:43], v[24:27]\n\t"
    "ds_read_b128 v[40:43], v3 offset:1088\n\t"
    "ds_read_b128 v[32:35], v2 offset:16448\n\t"
    "s_waitcnt lgkmcnt(2)\n\t"
    "v_mfma_f32_16x16x32_bf16 v[8:11], a[0:3], v[36:39], 0\n\t"
    "v_mfma_f32_16x16x32_bf16 v[12:15], a[32:35], v[36:39], 0\n\t"
    "v_mfma_f32_16x16x32_bf16 v[16:19], a[64:67], v[36:39], 0\n\t"
    "v_mfma_f32_16x16x32_bf16 v[20:23], v[28:31], v[36:39], 0\n\t"
    "ds_read_b128 v[36:39], v3 offset:1152\n\t"
    "ds_read_b128 v[28:31], v2 offset:17408\n\t"
    "s_waitcnt lgkmcnt(2)\n\t"
    "v_mfma_f32_16x16x32_bf16 v[8:11], a[4:7], v[40:43], v[8:11]\n\t"
    "v_mfma_f32_16x16x32_bf16 v[12:15], a[36:39], v[40:43], v[12:15]\n\t"
    "v_mfma_f32_16x16x32_bf16 v[16:19], a[68:71], v[40:43], v[16:19]\n\t"
    "v_mfma_f32_16x16x32_bf16 v[20:23], v[32:35], v[40:43], v[20:23]\n\t"
    "ds_read_b128 v[40:43], v3 offset:1216\n\t"
    "ds_read_b128 v[32:35], v2 offset:17472\n\t"
    "s_waitcnt lgkmcnt(2)\n\t"
    "v_mfma_f32_16x16x32_bf16 v[8:11], a[8:11], v[36:39], v[8:11]\n\t"
    "v_mfma_f32_16x16x32_bf16 v[12:15], a[40:43], v[36:39], v[12:15]\n\t"
    "v_mfma_f32_16x16x32_bf16 v[16:19], a[72:75], v[36:39], v[16:19]\n\t"
    "v_mfma_f32_16x16x32_bf16 v[20:23], v[28:31], v[36:39], v[20:23]\n\t"
    "ds_read_b128 v[36:39], v3 offset:1280\n\t"
    "ds_read_b128 v[28:31], v2 offset:18432\n\t"
    "v_cndmask_b32 v24, v24, v25, s[60:61]\n\t"
    "v_cndmask_b32 v26, v26, v27, s[60:61]\n\t"
    "v_cndmask_b32 v24, v24, v26, s[62:63]\n\t"
    "s_waitcnt lgkmcnt(2)\n\t"
    "v_mfma_f32_16x16x32_bf16 v[8:11], a[12:15], v[40:43], v[8:11]\n\t"
    "v_mfma_f32_16x16x32_bf16 v[12:15], a[44:47], v[40:43], v[12:15]\n\t"
    "v_mfma_f32_16x16x32_bf16 v[16:19], a[76:79], v[40:43], v[16:19]\n\t"
    "v_mfma_f32_16x16x32_bf16 v[20:23], v[32:35], v[40:43], v[20:23]\n\t"
    "ds_read_b128 v[40:43], v3 offset:1344\n\t"
    "ds_read_b128 v[32:35], v2 offset:18496\n\t"
    "v_add_f32_dpp v25, v24, v24 quad_perm:[1,0,3,2] row_mask:0xf bank_mask:0xf\n\t"
    "s_waitcnt lgkmcnt(2)\n\t"
    "v_mfma_f32_16x16x32_bf16 v[8:11], a[16:19], v[36:39], v[8:11]\n\t"
    "v_mfma_f32_16x16x32_bf16 v[12:15], a[48:51], v[36:39], v[12:15]\n\t"
    "v_mfma_f32_16x16x32_bf16 v[16:19], a[80:83], v[36:39], v[16:19]\n\t"
    "v_mfma_f32_16x16x32_bf16 v[20:23], v[28:31], v[36:39], v[20:23]\n\t"
    "ds_read_b128 v[36:39], v3 offset:1408\n\t"
    "ds_read_b128 v[28:31], v2 offset:19456\n\t"
    "v_add_f32_dpp v24, v25, v25 quad_perm:[2,3,0,1] row_mask:0xf bank_mask:0xf\n\t"
    "s_waitcnt lgkmcnt(2)\n\t"
    "v_mfma_f32_16x16x32_bf16 v[8:11], a[20:23], v[40:43], v[8:11]\n\t"
    "v_mfma_f32_16x16x32_bf16 v[12:15], a[52:55], v[40:43], v[12:15]\n\t"
    "v_mfma_f32_16x16x32_bf16 v[16:19], a[84:87], v[40:43], v[16:19]\n\t"
    "v_mfma_f32_16x16x32_bf16 v[20:23], v[32:35], v[40:43], v[20:23]\n\t"
    "ds_read_b128 v[40:43], v3 offset:1472\n\t"
    "ds_read_b128 v[32:35], v2 offset:19520\n\t"
    "s_waitcnt lgkmcnt(2)\n\t"
    "v_mfma_f32_16x16x32_bf16 v[8:11], a[24:27], v[36:39], v[8:11]\n\t"
    "v_mfma_f32_16x16x32_bf16 v[12:15], a[56:59], v[36:39], v[12:15]\n\t"
    "v_mfma_f32_16x16x32_bf16 v[16:19], a[88:91], v[36:39], v[16:19]\n\t"
    "v_mfma_f32_16x16x32_bf16 v[20:23], v[28:31], v[36:39], v[20:23]\n\t"
    "s_waitcnt lgkmcnt(0)\n\t"
    "v_mfma_f32_16x16x32_bf16 v[8:11], a[28:31], v[40:43], v[8:11]\n\t"
    "v_mfma_f32_16x16x32_bf16 v[12:15], a[60:63], v[40:43], v[12:15]\n\t"
    "v_mfma_f32_16x16x32_bf16 v[16:19], a[92:95], v[40:43], v[16:19]\n\t"
    "v_mfma_f32_16x16x32_bf16 v[20:23], v[32:35], v[40:43], v[20:23]\n\t"
    "s_nop 7\n\t"
    "s_add_u32 s53, s69, 0x6000\n\t"
    "s_add_u32 m0, s54, 0x4000\n\t"
    "s_nop 0\n\t"
    "buffer_load_dwordx4 v0, s[40:43], s53 offen offset:0 nt lds\n\t"
    "v_max3_f32 v44, v24, v8, v9\n\t"
    "v_max3_f32 v47, v10, v11, v12\n\t"
    "v_max3_f32 v44, v44, v13, v14\n\t"
    "v_max3_f32 v47, v47, v15, v16\n\t"
    "v_max3_f32 v44, v44, v17, v18\n\t"
    "v_max3_f32 v47, v47, v19, v20\n\t"
    "v_max3_f32 v44, v44, v21, v22\n\t"
    "v_max3_f32 v44, v44, v47, v23\n\t"
    "v_mov_b32 v46, v44\n\t"
    "s_nop 1\n\t"
    "v_permlane16_swap_b32 v44, v46\n\t"
    "v_max_f32 v44, v44, v46\n\t"
    "v_mov_b32 v46, v44\n\t"
    "s_nop 1\n\t"
    "v_permlane32_swap_b32 v44, v46\n\t"
    "v_max_f32 v44, v44, v46\n\t"
    "v_mul_f32 v44, s59, v44\n\t"
    "v_fma_f32 v8, v8, s59, -v44\n\t"
    "v_exp_f32 v8, v8\n\t"
    "v_fma_f32 v9, v9, s59, -v44\n\t"
    "v_exp_f32 v9, v9\n\t"
    "v_mov_b32 v45, v8\n\t"
    "v_cvt_pk_bf16_f32 v8, v8, v8\n\t"
    "v_and_b32 v36, v8, v4\n\t"
    "v_and_b32 v37, v8, v5\n\t"
    "v_and_b32 v38, v8, v6\n\t"
    "v_and_b32 v39, v8, v7\n\t"
    "v_fma_f32 v10, v10, s59, -v44\n\t"
    "v_exp_f32 v10, v10\n\t"
    "v_mfma_f32_16x16x32_bf16 v[28:31], v[36:39], a[96:99], 0\n\t"
    "v_add_f32 v45, v45, v9\n\t"
    "v_cvt_pk_bf16_f32 v9, v9, v9\n\t"
    "v_and_b32 v40, v9, v4\n\t"
    "v_and_b32 v41, v9, v5\n\t"
    "v_and_b32 v42, v9, v6\n\t"
    "v_and_b32 v43, v9, v7\n\t"
    "v_mfma_f32_16x16x32_bf16 v[32:35], v[36:39], a[100:103], 0\n\t"
    "v_fma_f32 v11, v11, s59, -v44\n\t"
    "v_exp_f32 v11, v11\n\t"
    "v_mfma_f32_16x16x32_bf16 v[28:31], v[40:43], a[104:107], v[28:31]\n\t"
    "v_add_f32 v45, v45, v10\n\t"
    "v_cvt_pk_bf16_f32 v10, v10, v10\n\t"
    "v_and_b32 v36, v10, v4\n\t"
    "v_and_b32 v37, v10, v5\n\t"
    "v_and_b32 v38, v10, v6\n\t"
    "v_and_b32 v39, v10, v7\n\t"
    "v_mfma_f32_16x16x32_bf16 v[32:35], v[40:43], a[108:111], v[32:35]\n\t"
    "v_fma_f32 v12, v12, s59, -v44\n\t"
    "v_exp_f32 v12, v12\n\t"
    "v_mfma_f32_16x16x32_bf16 v[28:31], v[36:39], a[112:115], v[28:31]\n\t"
    "v_add_f32 v45, v45, v11\n\t"
    "v_cvt_pk_bf16_f32 v11, v11, v11\n\t"
    "v_and_b32 v40, v11, v4\n\t"
    "v_and_b32 v41, v11, v5\n\t"
    "v_and_b32 v42, v11, v6\n\t"
    "v_and_b32 v43, v11, v7\n\t"
    "v_mfma_f32_16x16x32_bf16 v[32:35], v[36:39], a[116:119], v[32:35]\n\t"
    "s_add_u32 m0, s54, 0x4380\n\t"
    "s_nop 0\n\t"
    "buffer_load_dwordx4 v0, s[40:43], s53 offen offset:128 nt lds\n\t"
    "v_fma_f32 v13, v13, s59, -v44\n\t"
    "v_exp_f32 v13, v13\n\t"
    "v_mfma_f32_16x16x32_bf16 v[28:31], v[40:43], a[120:123], v[28:31]\n\t"
    "v_add_f32 v45, v45, v12\n\t"
    "v_cvt_pk_bf16_f32 v12, v12, v12\n\t"
    "v_and_b32 v36, v12, v4\n\t"
    "v_and_b32 v37, v12, v5\n\t"
    "v_and_b32 v38, v12, v6\n\t"
    "v_and_b32 v39, v12, v7\n\t"
    "v_mfma_f32_16x16x32_bf16 v[32:35], v[40:43], a[124:127], v[32:35]\n\t"
    "v_fma_f32 v14, v14, s59, -v44\n\t"
    "v_exp_f32 v14, v14\n\t"
    "v_mfma_f32_16x16x32_bf16 v[28:31], v[36:39], a[128:131], v[28:31]\n\t"
    "v_add_f32 v45, v45, v13\n\t"
    "v_cvt_pk_bf16_f32 v13, v13, v13\n\t"
    "v_and_b32 v40, v13, v4\n\t"
    "v_and_b32 v41, v13, v5\n\t"
    "v_and_b32 v42, v13, v6\n\t"
    "v_and_b32 v43, v13, v7\n\t"
    "v_mfma_f32_16x16x32_bf16 v[32:35], v[36:39], a[132:135], v[32:35]\n\t"
    "v_fma_f32 v15, v15, s59, -v44\n\t"
    "v_exp_f32 v15, v15\n\t"
    "v_mfma_f32_16x16x32_bf16 v[28:31], v[40:43], a[136:139], v[28:31]\n\t"
    "v_add_f32 v45, v45, v14\n\t"
    "v_cvt_pk_bf16_f32 v14, v14, v14\n\t"
    "v_and_b32 v36, v14, v4\n\t"
    "v_and_b32 v37, v14, v5\n\t"
    "v_and_b32 v38, v14, v6\n\t"
    "v_and_b32 v39, v14, v7\n\t"
    "v_mfma_f32_16x16x32_bf16 v[32:35], v[40:43], a[140:143], v[32:35]\n\t"
    "v_fma_f32 v16, v16, s59, -v44\n\t"
    "v_exp_f32 v16, v16\n\t"
    "v_mfma_f32_16x16x32_bf16 v[28:31], v[36:39], a[144:147], v[28:31]\n\t"
    "v_add_f32 v45, v45, v15\n\t"
    "v_cvt_pk_bf16_f32 v15, v15, v15\n\t"
    "v_and_b32 v40, v15, v4\n\t"
    "v_and_b32 v41, v15, v5\n\t"
    "v_and_b32 v42, v15, v6\n\t"
    "v_and_b32 v43, v15, v7\n\t"
    "v_mfma_f32_16x16x32_bf16 v[32:35], v[36:39], a[148:151], v[32:35]\n\t"
    "v_fma_f32 v17, v17, s59, -v44\n\t"
    "v_exp_f32 v17, v17\n\t"
    "v_mfma_f32_16x16x32_bf16 v[28:31], v[40:43], a[152:155], v[28:31]\n\t"
    "v_add_f32 v45, v45, v16\n\t"
    "v_cvt_pk_bf16_f32 v16, v16, v16\n\t"
    "v_and_b32 v36, v16, v4\n\t"
    "v_and_b32 v37, v16, v5\n\t"
    "v_and_b32 v38, v16, v6\n\t"
    "v_and_b32 v39, v16, v7\n\t"
    "v_mfma_f32_16x16x32_bf16 v[32:35], v[40:43], a[156:159], v[32:35]\n\t"
    "s_add_u32 m0, s54, 0x4700\n\t"
    "s_nop 0\n\t"
    "buffer_load_dwordx4 v0, s[40:43], s53 offen offset:256 nt lds\n\t"
    "v_fma_f32 v18, v18, s59, -v44\n\t"
    "v_exp_f32 v18, v18\n\t"
    "v_mfma_f32_16x16x32_bf16 v[28:31], v[36:39], a[160:163], v[28:31]\n\t"
    "v_add_f32 v45, v45, v17\n\t"
    "v_cvt_pk_bf16_f32 v17, v17, v17\n\t"
    "v_and_b32 v40, v17, v4\n\t"
    "v_and_b32 v41, v17, v5\n\t"
    "v_and_b32 v42, v17, v6\n\t"
    "v_and_b32 v43, v17, v7\n\t"
    "v_mfma_f32_16x16x32_bf16 v[32:35], v[36:39], a[164:167], v[32:35]\n\t"
    "v_fma_f32 v19, v19, s59, -v44\n\t"
    "v_exp_f32 v19, v19\n\t"
    "v_mfma_f32_16x16x32_bf16 v[28:31], v[40:43], a[168:171], v[28:31]\n\t"
    "v_add_f32 v45, v45, v18\n\t"
    "v_cvt_pk_bf16_f32 v18, v18, v18\n\t"
    "v_and_b32 v36, v18, v4\n\t"
    "v_and_b32 v37, v18, v5\n\t"
    "v_and_b32 v38, v18, v6\n\t"
    "v_and_b32 v39, v18, v7\n\t"
    "v_mfma_f32_16x16x32_bf16 v[32:35], v[40:43], a[172:175], v[32:35]\n\t"
    "v_fma_f32 v20, v20, s59, -v44\n\t"
    "v_exp_f32 v20, v20\n\t"
    "v_mfma_f32_16x16x32_bf16 v[28:31], v[36:39], a[176:179], v[28:31]\n\t"
    "v_add_f32 v45, v45, v19\n\t"
    "v_cvt_pk_bf16_f32 v19, v19, v19\n\t"
    "v_and_b32 v40, v19, v4\n\t"
    "v_and_b32 v41, v19, v5\n\t"
    "v_and_b32 v42, v19, v6\n\t"
    "v_and_b32 v43, v19, v7\n\t"
    "v_mfma_f32_16x16x32_bf16 v[32:35], v[36:39], a[180:183], v[32:35]\n\t"
    "v_fma_f32 v21, v21, s59, -v44\n\t"
    "v_exp_f32 v21, v21\n\t"
    "v_mfma_f32_16x16x32_bf16 v[28:31], v[40:43], a[184:187], v[28:31]\n\t"
    "v_add_f32 v45, v45, v20\n\t"
    "v_cvt_pk_bf16_f32 v20, v20, v20\n\t"
    "v_and_b32 v36, v20, v4\n\t"
    "v_and_b32 v37, v20, v5\n\t"
    "v_and_b32 v38, v20, v6\n\t"
    "v_and_b32 v39, v20, v7\n\t"
    "v_mfma_f32_16x16x32_bf16 v[32:35], v[40:43], a[188:191], v[32:35]\n\t"
    "v_fma_f32 v22, v22, s59, -v44\n\t"
    "v_exp_f32 v22, v22\n\t"
    "v_mfma_f32_16x16x32_bf16 v[28:31], v[36:39], a[192:195], v[28:31]\n\t"
    "v_add_f32 v45, v45, v21\n\t"
    "v_cvt_pk_bf16_f32 v21, v21, v21\n\t"
    "v_and_b32 v40, v21, v4\n\t"
    "v_and_b32 v41, v21, v5\n\t"
    "v_and_b32 v42, v21, v6\n\t"
    "v_and_b32 v43, v21, v7\n\t"
    "v_mfma_f32_16x16x32_bf16 v[32:35], v[36:39], a[196:199], v[32:35]\n\t"
    "s_add_u32 m0, s54, 0x4a80\n\t"
    "s_nop 0\n\t"
    "buffer_load_dwordx4 v0, s[40:43], s53 offen offset:384 nt lds\n\t"
    "v_fma_f32 v23, v23, s59, -v44\n\t"
    "v_exp_f32 v23, v23\n\t"
    "v_mfma_f32_16x16x32_bf16 v[28:31], v[40:43], a[200:203], v[28:31]\n\t"
    "v_add_f32 v45, v45, v22\n\t"
    "v_cvt_pk_bf16_f32 v22, v22, v22\n\t"
    "v_and_b32 v36, v22, v4\n\t"
    "v_and_b32 v37, v22, v5\n\t"
    "v_and_b32 v38, v22, v6\n\t"
    "v_and_b32 v39, v22, v7\n\t"
    "v_mfma_f32_16x16x32_bf16 v[32:35], v[40:43], a[204:207], v[32:35]\n\t"
    "v_fma_f32 v24, v24, s59, -v44\n\t"
    "v_exp_f32 v24, v24\n\t"
    "v_mfma_f32_16x16x32_bf16 v[28:31], v[36:39], a[208:211], v[28:31]\n\t"
    "v_add_f32 v45, v45, v23\n\t"
    "v_cvt_pk_bf16_f32 v23, v23, v23\n\t"
    "v_and_b32 v40, v23, v4\n\t"
    "v_and_b32 v41, v23, v5\n\t"
    "v_and_b32 v42, v23, v6\n\t"
    "v_and_b32 v43, v23, v7\n\t"
    "v_mfma_f32_16x16x32_bf16 v[32:35], v[36:39], a[212:215], v[32:35]\n\t"
    "s_nop 1\n\t"
    "v_mfma_f32_16x16x32_bf16 v[28:31], v[40:43], a[216:219], v[28:31]\n\t"
    "v_add_f32 v45, v45, v24\n\t"
    "v_cvt_pk_bf16_f32 v24, v24, v24\n\t"
    "v_and_b32 v36, v24, v4\n\t"
    "v_and_b32 v37, v24, v5\n\t"
    "v_and_b32 v38, v24, v6\n\t"
    "v_and_b32 v39, v24, v7\n\t"
    "v_mfma_f32_16x16x32_bf16 v[32:35], v[40:43], a[220:223], v[32:35]\n\t"
    "s_nop 1\n\t"
    "v_mfma_f32_16x16x32_bf16 v[28:31], v[36:39], a[224:227], v[28:31]\n\t"
    "v_mfma_f32_16x16x32_bf16 v[32:35], v[36:39], a[228:231], v[32:35]\n\t"
    "s_add_u32 s53, s69, 0x7000\n\t"
    "s_add_u32 m0, s54, 0x5000\n\t"
    "s_nop 0\n\t"
    "buffer_load_dwordx4 v0, s[40:43], s53 offen offset:0 nt lds\n\t"
    "v_mov_b32 v46, v45\n\t"
    "s_nop 1\n\t"
    "v_permlane16_swap_b32 v45, v46\n\t"
    "v_add_f32 v45, v45, v46\n\t"
    "v_mov_b32 v46, v45\n\t"
    "s_nop 1\n\t"
    "v_permlane32_swap_b32 v45, v46\n\t"
    "v_add_f32 v45, v45, v46\n\t"
    "v_mbcnt_lo_u32_b32 v46, -1, 0\n\t"
    "v_mbcnt_hi_u32_b32 v46, -1, v46\n\t"
    "v_lshlrev_b32 v46, 4, v46\n\t"
    "v_and_b32 v8, 0x2f0, v46\n\t"
    "v_lshrrev_b32 v9, 4, v46\n\t"
    "v_and_b32 v10, 16, v9\n\t"
    "v_lshl_add_u32 v8, v8, 1, v10\n\t"
    "v_add_u32 v8, s54, v8\n\t"
    "ds_write_b128 v8, v[28:31] offset:36864\n\t"
    "ds_write_b128 v8, v[32:35] offset:37376\n\t"
    "v_and_b32 v10, 8, v9\n\t"
    "v_lshl_add_u32 v10, v10, 2, s71\n\t"
    "ds_write_b64 v10, v[44:45] offset:2048\n\t"
    "v_and_b32 v26, 32, v9\n\t"
    "v_and_b32 v47, 31, v9\n\t"
    "v_lshlrev_b32 v24, 3, v47\n\t"
    "v_lshl_add_u32 v24, v26, 5, v24\n\t"
    "v_lshrrev_b32 v46, 1, v24\n\t"
    "v_add_u32 v24, s70, v24\n\t"
    "v_add_u32 v25, 0x13000, v24\n\t"
    "v_lshlrev_b32 v47, 3, v47\n\t"
    "v_lshl_add_u32 v47, v26, 1, v47\n\t"
    "s_waitcnt lgkmcnt(0)\n\t"
    "s_barrier\n\t"
    "ds_read_b128 v[16:19], v26 offset:2048\n\t"
    "ds_read_b128 v[20:23], v26 offset:2064\n\t"
    "ds_read_b64 v[8:9], v24 offset:0\n\t"
    "ds_read_b64 v[10:11], v24 offset:38912\n\t"
    "ds_read_b64 v[12:13], v25 offset:0\n\t"
    "ds_read_b64 v[14:15], v25 offset:38912\n\t"
    "s_and_b32 s58, s55, 1\n\t"
    "s_lshr_b32 s57, s55, 1\n\t"
    "s_add_u32 s57, s57, 1\n\t"
    "s_lshl_b32 s73, s58, 18\n\t"
    "s_lshl_b32 s58, s58, 15\n\t"
    "s_add_u32 s58, s58, s68\n\t"
    "s_lshl_b32 s79, s55, 18\n\t"
    "s_add_u32 s79, s79, 0x40000\n\t"
    "s_cmp_lt_u32 s55, 8\n\t"
    "s_cselect_b32 s73, s79, s73\n\t"
    "s_cselect_b32 s57, 1, s57\n\t"
    "s_add_u32 s73, s73, s67\n\t"
    "s_add_u32 m0, s54, 0x5380\n\t"
    "s_nop 0\n\t"
    "buffer_load_dwordx4 v0, s[40:43], s53 offen offset:128 nt lds\n\t"
    "s_waitcnt lgkmcnt(4)\n\t"
    "v_max3_f32 v44, v16, v18, v20\n\t"
    "v_max_f32 v44, v44, v22\n\t"
    "v_bfi_b32 v44, 1, s57, v44\n\t"
    "v_sub_f32 v16, v16, v44\n\t"
    "v_sub_f32 v18, v18, v44\n\t"
    "v_sub_f32 v20, v20, v44\n\t"
    "v_sub_f32 v22, v22, v44\n\t"
    "v_exp_f32 v16, v16\n\t"
    "v_exp_f32 v18, v18\n\t"
    "v_exp_f32 v20, v20\n\t"
    "v_exp_f32 v22, v22\n\t"
    "s_waitcnt lgkmcnt(0)\n\t"
    "s_barrier\n\t"
    "v_pk_mul_f32 v[8:9], v[8:9], v[16:17] op_sel_hi:[1,0]\n\t"
    "v_mul_f32 v45, v17, v16\n\t"
    "v_pk_fma_f32 v[8:9], v[10:11], v[18:19], v[8:9] op_sel_hi:[1,0,1]\n\t"
    "v_fmac_f32 v45, v19, v18\n\t"
    "v_pk_fma_f32 v[8:9], v[12:13], v[20:21], v[8:9] op_sel_hi:[1,0,1]\n\t"
    "v_fmac_f32 v45, v21, v20\n\t"
    "v_pk_fma_f32 v[8:9], v[14:15], v[22:23], v[8:9] op_sel_hi:[1,0,1]\n\t"
    "v_fmac_f32 v45, v23, v22\n\t"
    "v_cvt_pk_bf16_f32 v8, v8, v9\n\t"
    "v_bfi_b32 v45, 1, s57, v45\n\t"
    "v_bfi_b32 v8, 1, s57, v8\n\t"
    "s_sub_u32 s79, s55, 2\n\t"
    "s_cmp_lt_u32 s79, 5\n\t"
    "s_cbranch_scc0 .Lcs16_%=\n\t"
    "s_lshl_b32 s79, s79, 10\n\t"
    "s_lshl_b32 s73, s72, 7\n\t"
    "s_add_u32 s79, s79, s73\n\t"
    "v_add_u32 v9, s79, v46\n\t"
    "ds_write_b32 v9, v8 offset:3072\n\t"
    "s_branch .Lct16_%=\n\t"
    ".Lcs16_%=:\n\t"
    "buffer_store_dword v8, v46, s[48:51], s73 offen sc1\n\t"
    ".Lct16_%=:\n\t"
    "s_cmp_lg_u32 s72, 0\n\t"
    "s_cbranch_scc1 .Lcu16_%=\n\t"
    "s_cmp_lt_u32 s55, 8\n\t"
    "s_cbranch_scc0 .Lcv16_%=\n\t"
    "s_lshl_b32 s79, s55, 4\n\t"
    "v_lshrrev_b32 v9, 2, v26\n\t"
    "v_add_u32 v9, s79, v9\n\t"
    "ds_write_b64 v9, v[44:45] offset:2160\n\t"
    "s_branch .Lcu16_%=\n\t"
    ".Lcv16_%=:\n\t"
    "s_mov_b32 exec_lo, 0xff\n\t"
    "s_mov_b32 exec_hi, 0xff\n\t"
    "buffer_store_dwordx2 v[44:45], v47, s[48:51], s58 offen sc1\n\t"
    "s_mov_b64 exec, -1\n\t"
    ".Lcu16_%=:\n\t"
    "s_cmp_eq_u32 s55, 7\n\t"
    "s_cbranch_scc1 .Lflush_%=\n\t"
    "s_mov_b32 s52, s69\n\t"
    "s_add_u32 s55, s55, 1\n\t"
    "s_branch .Leven_%=\n\t"
    ".Lflush_%=:\n\t"
    "s_lshl_b32 s73, s72, 7\n\t"
    "v_add_u32 v47, s73, v46\n\t"
    "ds_read_b32 v8, v47 offset:3072\n\t"
    "ds_read_b32 v9, v47 offset:4096\n\t"
    "ds_read_b32 v10, v47 offset:5120\n\t"
    "ds_read_b32 v11, v47 offset:6144\n\t"
    "ds_read_b32 v12, v47 offset:7168\n\t"
    "s_add_u32 s73, s67, 0xc0000\n\t"
    "s_waitcnt lgkmcnt(4)\n\t"
    "buffer_store_dword v8, v46, s[48:51], s73 offen sc1\n\t"
    "s_add_u32 s73, s67, 0x100000\n\t"
    "s_waitcnt lgkmcnt(3)\n\t"
    "buffer_store_dword v9, v46, s[48:51], s73 offen sc1\n\t"
    "s_add_u32 s73, s67, 0x140000\n\t"
    "s_waitcnt lgkmcnt(2)\n\t"
    "buffer_store_dword v10, v46, s[48:51], s73 offen sc1\n\t"
    "s_add_u32 s73, s67, 0x180000\n\t"
    "s_waitcnt lgkmcnt(1)\n\t"
    "buffer_store_dword v11, v46, s[48:51], s73 offen sc1\n\t"
    "s_add_u32 s73, s67, 0x1c0000\n\t"
    "s_waitcnt lgkmcnt(0)\n\t"
    "buffer_store_dword v12, v46, s[48:51], s73 offen sc1\n\t"
    "v_mbcnt_lo_u32_b32 v46, -1, 0\n\t"
    "v_mbcnt_hi_u32_b32 v46, -1, v46\n\t"
    "v_lshlrev_b32 v46, 4, v46\n\t"
    "v_lshrrev_b32 v47, 1, v46\n\t"
    "ds_read_b64 v[14:15], v47 offset:2176\n\t"
    "s_add_u32 s73, s68, 0x10000\n\t"
    "s_waitcnt vmcnt(0) lgkmcnt(0)\n\t"
    "s_barrier\n\t"
    "s_cmp_lg_u32 s72, 0\n\t"
    "s_cbranch_scc1 .Lfl1_%=\n\t"
    "s_mov_b64 exec, 0xffff\n\t"
    "buffer_store_dwordx2 v[14:15], v47, s[48:51], s73 offen sc1\n\t"
    "s_mov_b64 exec, -1\n\t"
    ".Lfl1_%=:\n\t"
    "s_cmp_ge_u32 s78, 14\n\t"
    "s_cbranch_scc1 .Lfl9_%=\n\t"
    "v_mbcnt_lo_u32_b32 v0, -1, 0\n\t"
    "v_mbcnt_hi_u32_b32 v0, -1, v0\n\t"
    "v_lshrrev_b32 v1, 3, v0\n\t"
    "v_and_b32 v4, 7, v0\n\t"
    "v_lshlrev_b32 v4, 4, v4\n\t"
    "v_lshrrev_b32 v5, 4, v0\n\t"
    "v_lshl_add_u32 v6, v5, 2, v4\n\t"
    "v_accvgpr_write_b32 a0, v6\n\t"
    "v_lshl_add_u32 v0, v1, 10, v4\n\t"
    "v_lshlrev_b32 v1, 7, v1\n\t"
    "s_lshl_b32 s58, s78, 7\n\t"
    "s_sub_u32 s73, s73, s58\n\t"
    "s_lshl_b32 s58, s78, 3\n\t"
    "s_add_u32 s73, s73, s58\n\t"
    "s_lshl_b32 s58, s78, 10\n\t"
    "s_sub_u32 s79, s67, s58\n\t"
    "s_lshr_b32 s58, s78, 1\n\t"
    "s_lshl_b32 s58, s58, 18\n\t"
    "s_add_u32 s79, s79, s58\n\t"
    "s_and_b32 s58, s78, 1\n\t"
    "s_lshl_b32 s58, s58, 9\n\t"
    "s_add_u32 s79, s79, s58\n\t"
    "s_add_u32 s79, s79, 0x80000\n\t"
    ".Lfl2_%=:\n\t"
    "buffer_load_dwordx2 v[24:25], v1, s[48:51], s73 offen offset:0 sc1\n\t"
    "buffer_load_dwordx2 v[26:27], v1, s[48:51], s73 offen offset:1024 sc1\n\t"
    "buffer_load_dwordx2 v[28:29], v1, s[48:51], s73 offen offset:2048 sc1\n\t"
    "buffer_load_dwordx2 v[30:31], v1, s[48:51], s73 offen offset:3072 sc1\n\t"
    "s_waitcnt vmcnt(0)\n\t"
    "v_min3_u32 v4, v24, v25, v26\n\t"
    "v_min3_u32 v4, v4, v27, v28\n\t"
    "v_min3_u32 v4, v4, v29, v30\n\t"
    "v_min_u32 v4, v4, v31\n\t"
    "v_cmp_eq_u32 vcc, 0, v4\n\t"
    "s_cbranch_vccz .Lfl3_%=\n\t"
    "s_add_u32 s65, s65, 1\n\t"
    "s_cmp_gt_u32 s65, 0x40000\n\t"
    "s_cbranch_scc1 .Lfl3_%=\n\t"
    "s_sleep 2\n\t"
    "s_branch .Lfl2_%=\n\t"
    ".Lfl3_%=:\n\t"
    "buffer_load_dwordx4 v[8:11], v0, s[48:51], s79 offen sc1\n\t"
    "s_add_u32 s79, s79, 8192\n\t"
    "buffer_load_dwordx4 v[12:15], v0, s[48:51], s79 offen sc1\n\t"
    "s_add_u32 s79, s79, 8192\n\t"
    "buffer_load_dwordx4 v[16:19], v0, s[48:51], s79 offen sc1\n\t"
    "s_add_u32 s79, s79, 8192\n\t"
    "buffer_load_dwordx4 v[20:23], v0, s[48:51], s79 offen sc1\n\t"
    "s_add_u32 s79, s79, 8192\n\t"
    "v_max3_f32 v4, v24, v26, v28\n\t"
    "v_max_f32 v4, v4, v30\n\t"
    "s_nop 1\n\t"
    "v_max_f32_dpp v4, v4, v4 row_ror:8 row_mask:0xf bank_mask:0xf\n\t"
    "v_mov_b32 v5, v4\n\t"
    "s_nop 1\n\t"
    "v_permlane16_swap_b32 v4, v5\n\t"
    "v_max_f32 v4, v4, v5\n\t"
    "v_mov_b32 v5, v4\n\t"
    "s_nop 1\n\t"
    "v_permlane32_swap_b32 v4, v5\n\t"
    "v_max_f32 v4, v4, v5\n\t"
    "v_sub_f32 v24, v24, v4\n\t"
    "v_sub_f32 v26, v26, v4\n\t"
    "v_sub_f32 v28, v28, v4\n\t"
    "v_sub_f32 v30, v30, v4\n\t"
    "v_exp_f32 v24, v24\n\t"
    "v_exp_f32 v26, v26\n\t"
    "v_exp_f32 v28, v28\n\t"
    "v_exp_f32 v30, v30\n\t"
    "v_mul_f32 v6, v24, v25\n\t"
    "v_fmac_f32 v6, v26, v27\n\t"
    "v_fmac_f32 v6, v28, v29\n\t"
    "v_fmac_f32 v6, v30, v31\n\t"
    "s_waitcnt vmcnt(3)\n\t"
    "v_lshlrev_b32 v40, 16, v8\n\t"
    "v_and_b32 v41, 0xffff0000, v8\n\t"
    "v_lshlrev_b32 v42, 16, v9\n\t"
    "v_and_b32 v43, 0xffff0000, v9\n\t"
    "v_lshlrev_b32 v44, 16, v10\n\t"
    "v_and_b32 v45, 0xffff0000, v10\n\t"
    "v_lshlrev_b32 v46, 16, v11\n\t"
    "v_and_b32 v47, 0xffff0000, v11\n\t"
    "v_pk_mul_f32 v[32:33], v[40:41], v[24:25] op_sel_hi:[1,0]\n\t"
    "v_pk_mul_f32 v[34:35], v[42:43], v[24:25] op_sel_hi:[1,0]\n\t"
    "v_pk_mul_f32 v[36:37], v[44:45], v[24:25] op_sel_hi:[1,0]\n\t"
    "v_pk_mul_f32 v[38:39], v[46:47], v[24:25] op_sel_hi:[1,0]\n\t"
    "s_waitcnt vmcnt(2)\n\t"
    "v_lshlrev_b32 v40, 16, v12\n\t"
    "v_and_b32 v41, 0xffff0000, v12\n\t"
    "v_lshlrev_b32 v42, 16, v13\n\t"
    "v_and_b32 v43, 0xffff0000, v13\n\t"
    "v_lshlrev_b32 v44, 16, v14\n\t"
    "v_and_b32 v45, 0xffff0000, v14\n\t"
    "v_lshlrev_b32 v46, 16, v15\n\t"
    "v_and_b32 v47, 0xffff0000, v15\n\t"
    "v_pk_fma_f32 v[32:33], v[40:41], v[26:27], v[32:33] op_sel_hi:[1,0,1]\n\t"
    "v_pk_fma_f32 v[34:35], v[42:43], v[26:27], v[34:35] op_sel_hi:[1,0,1]\n\t"
    "v_pk_fma_f32 v[36:37], v[44:45], v[26:27], v[36:37] op_sel_hi:[1,0,1]\n\t"
    "v_pk_fma_f32 v[38:39], v[46:47], v[26:27], v[38:39] op_sel_hi:[1,0,1]\n\t"
    "s_waitcnt vmcnt(1)\n\t"
    "v_lshlrev_b32 v40, 16, v16\n\t"
    "v_and_b32 v41, 0xffff0000, v16\n\t"
    "v_lshlrev_b32 v42, 16, v17\n\t"
    "v_and_b32 v43, 0xffff0000, v17\n\t"
    "v_lshlrev_b32 v44, 16, v18\n\t"
    "v_and_b32 v45, 0xffff0000, v18\n\t"
    "v_lshlrev_b32 v46, 16, v19\n\t"
    "v_and_b32 v47, 0xffff0000, v19\n\t"
    "v_pk_fma_f32 v[32:33], v[40:41], v[28:29], v[32:33] op_sel_hi:[1,0,1]\n\t"
    "v_pk_fma_f32 v[34:35], v[42:43], v[28:29], v[34:35] op_sel_hi:[1,0,1]\n\t"
    "v_pk_fma_f32 v[36:37], v[44:45], v[28:29], v[36:37] op_sel_hi:[1,0,1]\n\t"
    "v_pk_fma_f32 v[38:39], v[46:47], v[28:29], v[38:39] op_sel_hi:[1,0,1]\n\t"
    "s_waitcnt vmcnt(0)\n\t"
    "v_lshlrev_b32 v40, 16, v20\n\t"
    "v_and_b32 v41, 0xffff0000, v20\n\t"
    "v_lshlrev_b32 v42, 16, v21\n\t"
    "v_and_b32 v43, 0xffff0000, v21\n\t"
    "v_lshlrev_b32 v44, 16, v22\n\t"
    "v_and_b32 v45, 0xffff0000, v22\n\t"
    "v_lshlrev_b32 v46, 16, v23\n\t"
    "v_and_b32 v47, 0xffff0000, v23\n\t"
    "v_pk_fma_f32 v[32:33], v[40:41], v[30:31], v[32:33] op_sel_hi:[1,0,1]\n\t"
    "v_pk_fma_f32 v[34:35], v[42:43], v[30:31], v[34:35] op_sel_hi:[1,0,1]\n\t"
    "v_pk_fma_f32 v[36:37], v[44:45], v[30:31], v[36:37] op_sel_hi:[1,0,1]\n\t"
    "v_pk_fma_f32 v[38:39], v[46:47], v[30:31], v[38:39] op_sel_hi:[1,0,1]\n\t"
    "v_mov_b32 v7, v6\n\t"
    "s_nop 0\n\t"
    "v_permlane32_swap_b32 v32, v36\n\t"
    "v_permlane32_swap_b32 v33, v37\n\t"
    "v_permlane32_swap_b32 v34, v38\n\t"
    "v_permlane32_swap_b32 v35, v39\n\t"
    "v_permlane32_swap_b32 v6, v7\n\t"
    "v_pk_add_f32 v[32:33], v[32:33], v[36:37]\n\t"
    "v_pk_add_f32 v[34:35], v[34:35], v[38:39]\n\t"
    "v_add_f32 v6, v6, v7\n\t"
    "v_mov_b32 v7, v6\n\t"
    "v_permlane16_swap_b32 v32, v34\n\t"
    "v_permlane16_swap_b32 v33, v35\n\t"
    "v_permlane16_swap_b32 v6, v7\n\t"
    "v_add_f32 v6, v6, v7\n\t"
    "v_pk_add_f32 v[32:33], v[32:33], v[34:35]\n\t"
    "s_nop 0\n\t"
    "v_add_f32_dpp v6, v6, v6 row_ror:8 row_mask:0xf bank_mask:0xf\n\t"
    "v_add_f32_dpp v32, v32, v32 row_ror:8 row_mask:0xf bank_mask:0xf\n\t"
    "v_rcp_f32 v6, v6\n\t"
    "v_add_f32_dpp v33, v33, v33 row_ror:8 row_mask:0xf bank_mask:0xf\n\t"
    "v_pk_mul_f32 v[32:33], v[32:33], v[6:7] op_sel_hi:[1,0]\n\t"
    "v_cvt_pk_bf16_f32 v32, v32, v33\n\t"
    "v_max_u32 v32, 1, v32\n\t"
    "v_accvgpr_read_b32 v4, a0\n\t"
    "s_lshr_b32 s73, s78, 1\n\t"
    "s_lshl_b32 s73, s73, 13\n\t"
    "s_add_u32 s73, s73, s58\n\t"
    "s_add_u32 s73, s73, s66\n\t"
    "s_lshl_b32 s58, s72, 7\n\t"
    "s_add_u32 s73, s73, s58\n\t"
    "s_add_u32 s73, s73, 0x96000\n\t"
    "s_mov_b32 exec_lo, 0x00ff00ff\n\t"
    "s_mov_b32 exec_hi, 0x00ff00ff\n\t"
    "buffer_store_dword v32, v4, s[48:51], s73 offen sc1\n\t"
    "s_mov_b64 exec, -1\n\t"
    "v_mbcnt_lo_u32_b32 v8, -1, 0\n\t"
    "v_mbcnt_hi_u32_b32 v8, -1, v8\n\t"
    "v_and_b32 v9, 1, v8\n\t"
    "v_lshlrev_b32 v9, 4, v9\n\t"
    "v_mov_b32 v11, 0xffff\n\t"
    "v_lshlrev_b32 v11, v9, v11\n\t"
    "v_bfe_u32 v9, v8, 1, 2\n\t"
    "v_cmp_eq_u32 vcc, 0, v9\n\t"
    "s_nop 1\n\t"
    "v_cndmask_b32 v4, 0, v11, vcc\n\t"
    "v_cmp_eq_u32 vcc, 1, v9\n\t"
    "s_nop 1\n\t"
    "v_cndmask_b32 v5, 0, v11, vcc\n\t"
    "v_cmp_eq_u32 vcc, 2, v9\n\t"
    "s_nop 1\n\t"
    "v_cndmask_b32 v6, 0, v11, vcc\n\t"
    "v_cmp_eq_u32 vcc, 3, v9\n\t"
    "s_nop 1\n\t"
    "v_cndmask_b32 v7, 0, v11, vcc\n\t"
    ".Lfl9_%=:\n\t"
    "s_mov_b32 s74, 1\n\t"
    "s_mov_b32 s55, 8\n\t"
    "s_branch .Ll2tail_%=\n\t"
    ".Lend_%=:\n\t"
    "s_endpgm\n\t"
    ".Ltab0_%=:\n\t"
    "s_lshr_b32 s88, s92, 1\n\t"
    "s_add_u32 s88, s88, 13952\n\t"
    "s_lshl_b32 s88, s88, 16\n\t"
    "s_lshl_b32 s89, s92, 31\n\t"
    "s_or_b32 s88, s88, s89\n\t"
    "v_pk_fma_f32 v[176:177], v[48:49], s[88:89], v[76:77] op_sel_hi:[1,0,1]\n\t"
    "v_pk_fma_f32 v[178:179], v[50:51], s[88:89], v[78:79] op_sel_hi:[1,0,1]\n\t"
    "v_pk_fma_f32 v[180:181], v[52:53], s[88:89], v[80:81] op_sel_hi:[1,0,1]\n\t"
    "v_pk_fma_f32 v[182:183], v[54:55], s[88:89], v[82:83] op_sel_hi:[1,0,1]\n\t"
    "v_pk_fma_f32 v[184:185], v[56:57], s[88:89], v[84:85] op_sel_hi:[1,0,1]\n\t"
    "v_pk_fma_f32 v[186:187], v[58:59], s[88:89], v[86:87] op_sel_hi:[1,0,1]\n\t"
    "v_pk_fma_f32 v[188:189], v[60:61], s[88:89], v[88:89] op_sel_hi:[1,0,1]\n\t"
    "v_pk_mul_f32 v[192:193], v[176:177], v[176:177]\n\t"
    "v_pk_mul_f32 v[194:195], v[178:179], v[178:179]\n\t"
    "v_pk_mul_f32 v[196:197], v[180:181], v[180:181]\n\t"
    "v_pk_mul_f32 v[198:199], v[182:183], v[182:183]\n\t"
    "v_pk_mul_f32 v[200:201], v[184:185], v[184:185]\n\t"
    "v_pk_mul_f32 v[202:203], v[186:187], v[186:187]\n\t"
    "v_pk_mul_f32 v[204:205], v[188:189], v[188:189]\n\t"
    "v_pk_fma_f32 v[192:193], v[192:193], s[90:91], v[210:211] op_sel_hi:[1,0,1]\n\t"
    "v_pk_fma_f32 v[194:195], v[194:195], s[90:91], v[210:211] op_sel_hi:[1,0,1]\n\t"
    "v_pk_fma_f32 v[196:197], v[196:197], s[90:91], v[210:211] op_sel_hi:[1,0,1]\n\t"
    "v_pk_fma_f32 v[198:199], v[198:199], s[90:91], v[210:211] op_sel_hi:[1,0,1]\n\t"
    "v_pk_fma_f32 v[200:201], v[200:201], s[90:91], v[210:211] op_sel_hi:[1,0,1]\n\t"
    "v_pk_fma_f32 v[202:203], v[202:203], s[90:91], v[210:211] op_sel_hi:[1,0,1]\n\t"
    "v_pk_fma_f32 v[204:205], v[204:205], s[90:91], v[210:211] op_sel_hi:[1,0,1]\n\t"
    "v_pk_mul_f32 v[192:193], v[192:193], v[176:177]\n\t"
    "v_pk_mul_f32 v[194:195], v[194:195], v[178:179]\n\t"
    "v_pk_mul_f32 v[196:197], v[196:197], v[180:181]\n\t"
    "v_pk_mul_f32 v[198:199], v[198:199], v[182:183]\n\t"
    "v_pk_mul_f32 v[200:201], v[200:201], v[184:185]\n\t"
    "v_pk_mul_f32 v[202:203], v[202:203], v[186:187]\n\t"
    "v_pk_mul_f32 v[204:205], v[204:205], v[188:189]\n\t"
    "v_exp_f32 v192, v192\n\t"
    "v_exp_f32 v193, v193\n\t"
    "v_exp_f32 v194, v194\n\t"
    "v_exp_f32 v195, v195\n\t"
    "v_exp_f32 v196, v196\n\t"
    "v_exp_f32 v197, v197\n\t"
    "v_exp_f32 v198, v198\n\t"
    "v_exp_f32 v199, v199\n\t"
    "v_exp_f32 v200, v200\n\t"
    "v_exp_f32 v201, v201\n\t"
    "v_exp_f32 v202, v202\n\t"
    "v_exp_f32 v203, v203\n\t"
    "v_exp_f32 v204, v204\n\t"
    "v_exp_f32 v205, v205\n\t"
    "v_pk_add_f32 v[192:193], v[192:193], 1.0 op_sel_hi:[1,0]\n\t"
    "v_pk_add_f32 v[194:195], v[194:195], 1.0 op_sel_hi:[1,0]\n\t"
    "v_pk_add_f32 v[196:197], v[196:197], 1.0 op_sel_hi:[1,0]\n\t"
    "v_pk_add_f32 v[198:199], v[198:199], 1.0 op_sel_hi:[1,0]\n\t"
    "v_pk_add_f32 v[200:201], v[200:201], 1.0 op_sel_hi:[1,0]\n\t"
    "v_pk_add_f32 v[202:203], v[202:203], 1.0 op_sel_hi:[1,0]\n\t"
    "v_pk_add_f32 v[204:205], v[204:205], 1.0 op_sel_hi:[1,0]\n\t"
    "v_rcp_f32 v192, v192\n\t"
    "v_rcp_f32 v193, v193\n\t"
    "v_rcp_f32 v194, v194\n\t"
    "v_rcp_f32 v195, v195\n\t"
    "v_rcp_f32 v196, v196\n\t"
    "v_rcp_f32 v197, v197\n\t"
    "v_rcp_f32 v198, v198\n\t"
    "v_rcp_f32 v199, v199\n\t"
    "v_rcp_f32 v200, v200\n\t"
    "v_rcp_f32 v201, v201\n\t"
    "v_rcp_f32 v202, v202\n\t"
    "v_rcp_f32 v203, v203\n\t"
    "v_rcp_f32 v204, v204\n\t"
    "v_rcp_f32 v205, v205\n\t"
    "v_pk_mul_f32 v[176:177], v[176:177], v[104:105]\n\t"
    "v_pk_mul_f32 v[178:179], v[178:179], v[106:107]\n\t"
    "v_pk_mul_f32 v[180:181], v[180:181], v[108:109]\n\t"
    "v_pk_mul_f32 v[182:183], v[182:183], v[110:111]\n\t"
    "v_pk_mul_f32 v[184:185], v[184:185], v[112:113]\n\t"
    "v_pk_mul_f32 v[186:187], v[186:187], v[114:115]\n\t"
    "v_pk_mul_f32 v[188:189], v[188:189], v[116:117]\n\t"
    "v_pk_mul_f32 v[208:209], v[176:177], v[192:193]\n\t"
    "v_pk_fma_f32 v[208:209], v[178:179], v[194:195], v[208:209]\n\t"
    "v_pk_fma_f32 v[208:209], v[180:181], v[196:197], v[208:209]\n\t"
    "v_pk_fma_f32 v[208:209], v[182:183], v[198:199], v[208:209]\n\t"
    "v_pk_fma_f32 v[208:209], v[184:185], v[200:201], v[208:209]\n\t"
    "v_pk_fma_f32 v[208:209], v[186:187], v[202:203], v[208:209]\n\t"
    "v_pk_fma_f32 v[208:209], v[188:189], v[204:205], v[208:209]\n\t"
    "s_setpc_b64 s[94:95]\n\t"
    ".Ltab1_%=:\n\t"
    "v_pk_fma_f32 v[176:177], v[62:63], s[88:89], v[90:91] op_sel_hi:[1,0,1]\n\t"
    "v_pk_fma_f32 v[178:179], v[64:65], s[88:89], v[92:93] op_sel_hi:[1,0,1]\n\t"
    "v_pk_fma_f32 v[180:181], v[66:67], s[88:89], v[94:95] op_sel_hi:[1,0,1]\n\t"
    "v_pk_fma_f32 v[182:183], v[68:69], s[88:89], v[96:97] op_sel_hi:[1,0,1]\n\t"
    "v_pk_fma_f32 v[184:185], v[70:71], s[88:89], v[98:99] op_sel_hi:[1,0,1]\n\t"
    "v_pk_fma_f32 v[186:187], v[72:73], s[88:89], v[100:101] op_sel_hi:[1,0,1]\n\t"
    "v_pk_fma_f32 v[188:189], v[74:75], s[88:89], v[102:103] op_sel_hi:[1,0,1]\n\t"
    "v_pk_mul_f32 v[192:193], v[176:177], v[176:177]\n\t"
    "v_pk_mul_f32 v[194:195], v[178:179], v[178:179]\n\t"
    "v_pk_mul_f32 v[196:197], v[180:181], v[180:181]\n\t"
    "v_pk_mul_f32 v[198:199], v[182:183], v[182:183]\n\t"
    "v_pk_mul_f32 v[200:201], v[184:185], v[184:185]\n\t"
    "v_pk_mul_f32 v[202:203], v[186:187], v[186:187]\n\t"
    "v_pk_mul_f32 v[204:205], v[188:189], v[188:189]\n\t"
    "v_pk_fma_f32 v[192:193], v[192:193], s[90:91], v[210:211] op_sel_hi:[1,0,1]\n\t"
    "v_pk_fma_f32 v[194:195], v[194:195], s[90:91], v[210:211] op_sel_hi:[1,0,1]\n\t"
    "v_pk_fma_f32 v[196:197], v[196:197], s[90:91], v[210:211] op_sel_hi:[1,0,1]\n\t"
    "v_pk_fma_f32 v[198:199], v[198:199], s[90:91], v[210:211] op_sel_hi:[1,0,1]\n\t"
    "v_pk_fma_f32 v[200:201], v[200:201], s[90:91], v[210:211] op_sel_hi:[1,0,1]\n\t"
    "v_pk_fma_f32 v[202:203], v[202:203], s[90:91], v[210:211] op_sel_hi:[1,0,1]\n\t"
    "v_pk_fma_f32 v[204:205], v[204:205], s[90:91], v[210:211] op_sel_hi:[1,0,1]\n\t"
    "v_pk_mul_f32 v[192:193], v[192:193], v[176:177]\n\t"
    "v_pk_mul_f32 v[194:195], v[194:195], v[178:179]\n\t"
    "v_pk_mul_f32 v[196:197], v[196:197], v[180:181]\n\t"
    "v_pk_mul_f32 v[198:199], v[198:199], v[182:183]\n\t"
    "v_pk_mul_f32 v[200:201], v[200:201], v[184:185]\n\t"
    "v_pk_mul_f32 v[202:203], v[202:203], v[186:187]\n\t"
    "v_pk_mul_f32 v[204:205], v[204:205], v[188:189]\n\t"
    "v_exp_f32 v192, v192\n\t"
    "v_exp_f32 v193, v193\n\t"
    "v_exp_f32 v194, v194\n\t"
    "v_exp_f32 v195, v195\n\t"
    "v_exp_f32 v196, v196\n\t"
    "v_exp_f32 v197, v197\n\t"
    "v_exp_f32 v198, v198\n\t"
    "v_exp_f32 v199, v199\n\t"
    "v_exp_f32 v200, v200\n\t"
    "v_exp_f32 v201, v201\n\t"
    "v_exp_f32 v202, v202\n\t"
    "v_exp_f32 v203, v203\n\t"
    "v_exp_f32 v204, v204\n\t"
    "v_exp_f32 v205, v205\n\t"
    "v_pk_add_f32 v[192:193], v[192:193], 1.0 op_sel_hi:[1,0]\n\t"
    "v_pk_add_f32 v[194:195], v[194:195], 1.0 op_sel_hi:[1,0]\n\t"
    "v_pk_add_f32 v[196:197], v[196:197], 1.0 op_sel_hi:[1,0]\n\t"
    "v_pk_add_f32 v[198:199], v[198:199], 1.0 op_sel_hi:[1,0]\n\t"
    "v_pk_add_f32 v[200:201], v[200:201], 1.0 op_sel_hi:[1,0]\n\t"
    "v_pk_add_f32 v[202:203], v[202:203], 1.0 op_sel_hi:[1,0]\n\t"
    "v_pk_add_f32 v[204:205], v[204:205], 1.0 op_sel_hi:[1,0]\n\t"
    "v_rcp_f32 v192, v192\n\t"
    "v_rcp_f32 v193, v193\n\t"
    "v_rcp_f32 v194, v194\n\t"
    "v_rcp_f32 v195, v195\n\t"
    "v_rcp_f32 v196, v196\n\t"
    "v_rcp_f32 v197, v197\n\t"
    "v_rcp_f32 v198, v198\n\t"
    "v_rcp_f32 v199, v199\n\t"
    "v_rcp_f32 v200, v200\n\t"
    "v_rcp_f32 v201, v201\n\t"
    "v_rcp_f32 v202, v202\n\t"
    "v_rcp_f32 v203, v203\n\t"
    "v_rcp_f32 v204, v204\n\t"
    "v_rcp_f32 v205, v205\n\t"
    "v_pk_mul_f32 v[176:177], v[176:177], v[118:119]\n\t"
    "v_pk_mul_f32 v[178:179], v[178:179], v[120:121]\n\t"
    "v_pk_mul_f32 v[180:181], v[180:181], v[122:123]\n\t"
    "v_pk_mul_f32 v[182:183], v[182:183], v[124:125]\n\t"
    "v_pk_mul_f32 v[184:185], v[184:185], v[126:127]\n\t"
    "v_pk_mul_f32 v[186:187], v[186:187], v[128:129]\n\t"
    "v_pk_mul_f32 v[188:189], v[188:189], v[130:131]\n\t"
    "v_pk_fma_f32 v[208:209], v[176:177], v[192:193], v[208:209]\n\t"
    "v_pk_fma_f32 v[208:209], v[178:179], v[194:195], v[208:209]\n\t"
    "v_pk_fma_f32 v[208:209], v[180:181], v[196:197], v[208:209]\n\t"
    "v_pk_fma_f32 v[208:209], v[182:183], v[198:199], v[208:209]\n\t"
    "v_pk_fma_f32 v[208:209], v[184:185], v[200:201], v[208:209]\n\t"
    "v_pk_fma_f32 v[208:209], v[186:187], v[202:203], v[208:209]\n\t"
    "v_pk_fma_f32 v[208:209], v[188:189], v[204:205], v[208:209]\n\t"
    "v_add_f32 v208, v208, v209\n\t"
    "s_nop 1\n\t"
    "v_add_f32_dpp v208, v208, v208 quad_perm:[1,0,3,2] row_mask:0xf bank_mask:0xf\n\t"
    "s_nop 1\n\t"
    "v_add_f32_dpp v208, v208, v208 quad_perm:[2,3,0,1] row_mask:0xf bank_mask:0xf\n\t"
    "s_nop 1\n\t"
    "v_add_f32_dpp v208, v208, v208 row_half_mirror row_mask:0xf bank_mask:0xf\n\t"
    "s_nop 1\n\t"
    "v_add_f32_dpp v208, v208, v208 row_mirror row_mask:0xf bank_mask:0xf\n\t"
    "v_cmp_eq_u32 vcc, s86, v212\n\t"
    "v_mov_b32 v46, v208\n\t"
    "s_nop 1\n\t"
    "v_permlane16_swap_b32 v208, v46\n\t"
    "v_add_f32 v208, v208, v46\n\t"
    "v_mov_b32 v46, v208\n\t"
    "s_nop 1\n\t"
    "v_permlane32_swap_b32 v208, v46\n\t"
    "v_add_f32 v208, v208, v46\n\t"
    "v_cndmask_b32 v14, v14, v208, vcc\n\t"
    "s_add_u32 s86, s86, 1\n\t"
    "s_add_u32 s92, s92, 1\n\t"
    "s_cmp_lt_u32 s86, 6\n\t"
    "s_cbranch_scc1 .Ltabr_%=\n\t"
    "v_lshlrev_b32 v213, 16, v213\n\t"
    "v_add_f32 v14, v14, v213\n\t"
    "v_max_u32 v14, 1, v14\n\t"
    "v_lshlrev_b32 v212, 2, v212\n\t"
    "s_lshl_b32 s88, s92, 2\n\t"
    "s_add_u32 s88, s88, 0x1ffe8\n\t"
    "s_mov_b64 exec, 63\n\t"
    "buffer_store_dword v14, v212, s[48:51], s88 offen sc1\n\t"
    "s_mov_b64 exec, -1\n\t"
    ".Ltabr_%=:\n\t"
    "s_mov_b64 vcc, -1\n\t"
    "s_setpc_b64 s[94:95]\n\t"
      :
      : "s"(p.K), "s"(p.V), "s"(p.ws), "s"(row0), "s"(W), "s"(qoff), "s"(accoff), "s"(mloff), "s"(split), "s"(wave), "s"(profoff),
        "s"(p.w1), "s"(p.b1), "s"(p.w2), "s"(p.b2), "s"(p.wq), "s"(p.wo), "s"(sidx * 4 + wave), "s"(g), "s"(split * 4 + wave)
      :
      "v255", "a255", "s40", "s41", "s42", "s43", "s44", "s45", "s46", "s47", "s48", "s49", "s50", "s51", "s52", "s53",
      "s54", "s55", "s56", "s57", "s58", "s59", "s60", "s61", "s62", "s63", "s64", "s65", "s66", "s67", "s68", "s69",
      "s70", "s71", "s72", "s73", "s74", "s75", "s76", "s77", "s78", "s79", "s80", "s81", "s82", "s83", "s84", "s85",
      "s86", "s87", "s88", "s89", "s90", "s91", "s92", "s93", "s94", "s95", "s96", "s97", "s98", "s99", "vcc", "scc",
      "memory");
}

DI void chain_role(const Params& p) {
  const u32 w = rfl(threadIdx.x >> 6);
  const u32 h = 2 * (blockIdx.x & 7) + (blockIdx.x >> 3);
  const u32 profoff = rfl(WS_PROF + (h * 4 + w) * 1024);
  asm volatile(
    "s_mov_b64 s[40:41], %0\n\t"
    "s_and_b32 s41, s41, 0xffff\n\t"
    "s_mov_b32 s42, 0x2000000\n\t"
    "s_mov_b32 s43, 0x20000\n\t"
    "s_mov_b32 s53, %11\n\t"
    "s_mov_b32 s52, %12\n\t"
    "s_mov_b32 s67, %13\n\t"
    "s_mov_b32 s58, 0\n\t"
    "s_lshr_b32 s36, s53, 1\n\t"
    "s_lshl_b32 s36, s36, 12\n\t"
    "s_and_b32 s63, s53, 1\n\t"
    "s_lshl_b32 s63, s63, 6\n\t"
    "s_add_u32 s36, s36, s63\n\t"
    "s_mov_b32 s72, -1\n\t"
    "s_mov_b32 s73, 0xffffff\n\t"
    "s_mov_b32 s66, 0x3db8aa3b\n\t"
    "v_mbcnt_lo_u32_b32 v0, -1, 0\n\t"
    "v_mbcnt_hi_u32_b32 v0, -1, v0\n\t"
    "s_mov_b64 s[48:49], %1\n\t"
    "s_and_b32 s49, s49, 0xffff\n\t"
    "s_mov_b32 s50, 0x1c00\n\t"
    "s_mov_b32 s51, 0x20000\n\t"
    "s_mov_b64 s[44:45], %2\n\t"
    "s_and_b32 s45, s45, 0xffff\n\t"
    "s_mov_b32 s46, 0x380000\n\t"
    "s_mov_b32 s47, 0x20000\n\t"
    "v_mov_b32 v3, 0\n\t"
    "s_mov_b32 s63, 3137536\n\t"
    "buffer_load_dwordx2 v[40:41], v3, s[40:43], s63 offen sc1\n\t"
    "global_load_ushort v3, v3, %9\n\t"
    "v_lshlrev_b32 v4, 4, v0\n\t"
    "s_mul_i32 s63, s52, 1792\n\t"
    "buffer_load_dwordx4 v[8:11], v4, s[48:51], s63 offen\n\t"
    "buffer_load_dwordx4 v[12:15], v4, s[48:51], s63 offen offset:896\n\t"
    "v_mov_b32 v44, 0\n\t"
    "v_mov_b32 v45, 0\n\t"
    "v_mov_b32 v46, 0\n\t"
    "v_mov_b32 v47, 0\n\t"
    "v_and_b32 v5, 31, v0\n\t"
    "v_lshlrev_b32 v5, 4, v5\n\t"
    "v_lshrrev_b32 v6, 5, v0\n\t"
    "v_lshl_add_u32 v5, v6, 13, v5\n\t"
    "s_lshl_b32 s64, s52, 15\n\t"
    "s_lshl_b32 s65, s53, 9\n\t"
    "s_add_u32 s64, s64, s65\n\t"
    "buffer_store_dwordx4 v[44:47], v5, s[40:43], s64 offen sc1\n\t"
    "s_add_u32 s64, s64, 16384\n\t"
    "buffer_store_dwordx4 v[44:47], v5, s[40:43], s64 offen sc1\n\t"
    "s_lshl_b32 s64, s53, 2\n\t"
    "s_add_u32 s64, s64, s52\n\t"
    "s_mul_i32 s64, s64, 384\n\t"
    "s_add_u32 s64, s64, 131072\n\t"
    "s_mov_b64 exec, 0xffffff\n\t"
    "buffer_store_dwordx4 v[44:47], v4, s[40:43], s64 offen sc1\n\t"
    "s_mov_b64 exec, 0xfffffff\n\t"
    "s_mul_i32 s33, s53, 1792\n\t"
    "s_mul_i32 s64, s52, 448\n\t"
    "s_add_u32 s33, s33, s64\n\t"
    "s_add_u32 s33, s33, 155648\n\t"
    "s_add_u32 s64, s33, 0\n\t"
    "buffer_store_dwordx4 v[44:47], v4, s[40:43], s64 offen sc1\n\t"
    "s_add_u32 s64, s33, 28672\n\t"
    "buffer_store_dwordx4 v[44:47], v4, s[40:43], s64 offen sc1\n\t"
    "s_add_u32 s64, s33, 430080\n\t"
    "buffer_store_dwordx4 v[44:47], v4, s[40:43], s64 offen sc1\n\t"
    "s_mov_b64 exec, 0xffff\n\t"
    "s_and_b32 s64, s36, 0xf000\n\t"
    "s_add_u32 s64, s64, 749312\n\t"
    "buffer_store_dwordx4 v[44:47], v4, s[40:43], s64 offen sc1\n\t"
    "s_mov_b64 exec, -1\n\t"
    "v_lshrrev_b32 v5, 3, v0\n\t"
    "v_and_b32 v6, 7, v0\n\t"
    "v_lshlrev_b32 v6, 4, v6\n\t"
    "v_lshl_add_u32 v5, v5, 13, v6\n\t"
    "s_lshl_b32 s60, s53, 9\n\t"
    "s_lshl_b32 s64, s52, 7\n\t"
    "s_add_u32 s60, s60, s64\n\t"
    "s_lshl_b32 s61, s52, 14\n\t"
    "s_add_u32 s61, s61, 29696\n\t"
    "s_mov_b32 s65, 0x10000\n\t"
    "s_add_u32 m0, s61, 0\n\t"
    "s_nop 0\n\t"
    "buffer_load_dwordx4 v5, s[44:47], s60 offen nt lds\n\t"
    "s_add_u32 s60, s60, s65\n\t"
    "s_add_u32 m0, s61, 1024\n\t"
    "s_nop 0\n\t"
    "buffer_load_dwordx4 v5, s[44:47], s60 offen nt lds\n\t"
    "s_add_u32 s60, s60, s65\n\t"
    "s_add_u32 m0, s61, 2048\n\t"
    "s_nop 0\n\t"
    "buffer_load_dwordx4 v5, s[44:47], s60 offen nt lds\n\t"
    "s_add_u32 s60, s60, s65\n\t"
    "s_add_u32 m0, s61, 3072\n\t"
    "s_nop 0\n\t"
    "buffer_load_dwordx4 v5, s[44:47], s60 offen nt lds\n\t"
    "s_add_u32 s60, s60, s65\n\t"
    "s_add_u32 m0, s61, 4096\n\t"
    "s_nop 0\n\t"
    "buffer_load_dwordx4 v5, s[44:47], s60 offen nt lds\n\t"
    "s_add_u32 s60, s60, s65\n\t"
    "s_add_u32 m0, s61, 5120\n\t"
    "s_nop 0\n\t"
    "buffer_load_dwordx4 v5, s[44:47], s60 offen nt lds\n\t"
    "s_add_u32 s60, s60, s65\n\t"
    "s_add_u32 m0, s61, 6144\n\t"
    "s_nop 0\n\t"
    "buffer_load_dwordx4 v5, s[44:47], s60 offen nt lds\n\t"
    "s_add_u32 s60, s60, s65\n\t"
    "s_add_u32 m0, s61, 7168\n\t"
    "s_nop 0\n\t"
    "buffer_load_dwordx4 v5, s[44:47], s60 offen nt lds\n\t"
    "s_add_u32 s60, s60, s65\n\t"
    "s_add_u32 m0, s61, 8192\n\t"
    "s_nop 0\n\t"
    "buffer_load_dwordx4 v5, s[44:47], s60 offen nt lds\n\t"
    "s_add_u32 s60, s60, s65\n\t"
    "s_add_u32 m0, s61, 9216\n\t"
    "s_nop 0\n\t"
    "buffer_load_dwordx4 v5, s[44:47], s60 offen nt lds\n\t"
    "s_add_u32 s60, s60, s65\n\t"
    "s_add_u32 m0, s61, 10240\n\t"
    "s_nop 0\n\t"
    "buffer_load_dwordx4 v5, s[44:47], s60 offen nt lds\n\t"
    "s_add_u32 s60, s60, s65\n\t"
    "s_add_u32 m0, s61, 11264\n\t"
    "s_nop 0\n\t"
    "buffer_load_dwordx4 v5, s[44:47], s60 offen nt lds\n\t"
    "s_add_u32 s60, s60, s65\n\t"
    "s_add_u32 m0, s61, 12288\n\t"
    "s_nop 0\n\t"
    "buffer_load_dwordx4 v5, s[44:47], s60 offen nt lds\n\t"
    "s_add_u32 s60, s60, s65\n\t"
    "s_add_u32 m0, s61, 13312\n\t"
    "s_nop 0\n\t"
    "buffer_load_dwordx4 v5, s[44:47], s60 offen nt lds\n\t"
    "s_add_u32 s60, s60, s65\n\t"
    "s_add_u32 m0, s61, 14336\n\t"
    "s_nop 0\n\t"
    "buffer_load_dwordx4 v5, s[44:47], s60 offen nt lds\n\t"
    "s_add_u32 s60, s60, s65\n\t"
    "s_add_u32 m0, s61, 15360\n\t"
    "s_nop 0\n\t"
    "buffer_load_dwordx4 v5, s[44:47], s60 offen nt lds\n\t"
    "s_add_u32 s60, s60, s65\n\t"
    "v_lshrrev_b32 v6, 4, v0\n\t"
    "v_lshlrev_b32 v6, 10, v6\n\t"
    "v_bfe_u32 v7, v0, 2, 2\n\t"
    "v_lshl_add_u32 v6, v7, 7, v6\n\t"
    "v_and_b32 v7, 3, v0\n\t"
    "v_lshl_add_u32 v6, v7, 5, v6\n\t"
    "v_add_u32 v6, s61, v6\n\t"
    "v_add_u32 v4, s63, v4\n\t"
    "s_waitcnt vmcnt(16)\n\t"
    "s_barrier\n\t"
    "v_readfirstlane_b32 s34, v40\n\t"
    "v_readfirstlane_b32 s35, v41\n\t"
    "s_add_u32 s34, s34, 1\n\t"
    "s_addc_u32 s35, s35, 0\n\t"
    "s_cmp_lg_u32 s52, 0\n\t"
    "s_cbranch_scc1 .Lc_f1_%=\n\t"
    "v_mov_b32 v40, s34\n\t"
    "v_mov_b32 v41, s35\n\t"
    "s_lshl_b32 s64, s53, 3\n\t"
    "s_add_u32 s64, s64, 3137792\n\t"
    "s_mov_b64 exec, 1\n\t"
    "buffer_store_dwordx2 v[40:41], v44, s[40:43], s64 offen sc1\n\t"
    "s_mov_b64 exec, -1\n\t"
    ".Lc_f1_%=:\n\t"
    "v_lshlrev_b32 v3, 16, v3\n\t"
    "s_nop 0\n\t"
    "v_readfirstlane_b32 s96, v3\n\t"
    "s_mov_b64 exec, s[72:73]\n\t"
    "ds_write_b128 v4, v[8:11] offset:7168\n\t"
    "s_mov_b64 exec, -1\n\t"
    "v_lshlrev_b32 v16, 16, v8\n\t"
    "v_and_b32 v17, 0xffff0000, v8\n\t"
    "v_lshlrev_b32 v18, 16, v9\n\t"
    "v_and_b32 v19, 0xffff0000, v9\n\t"
    "v_lshlrev_b32 v20, 16, v10\n\t"
    "v_and_b32 v21, 0xffff0000, v10\n\t"
    "v_lshlrev_b32 v22, 16, v11\n\t"
    "v_and_b32 v23, 0xffff0000, v11\n\t"
    "s_nop 0\n\t"
    "v_readfirstlane_b32 s74, v16\n\t"
    "s_nop 1\n\t"
    "v_pk_add_f32 v[24:25], v[16:17], s[74:75] op_sel_hi:[1,0] neg_lo:[0,1] neg_hi:[0,1]\n\t"
    "v_pk_add_f32 v[26:27], v[18:19], s[74:75] op_sel_hi:[1,0] neg_lo:[0,1] neg_hi:[0,1]\n\t"
    "v_pk_add_f32 v[28:29], v[20:21], s[74:75] op_sel_hi:[1,0] neg_lo:[0,1] neg_hi:[0,1]\n\t"
    "v_pk_add_f32 v[30:31], v[22:23], s[74:75] op_sel_hi:[1,0] neg_lo:[0,1] neg_hi:[0,1]\n\t"
    "v_pk_add_f32 v[32:33], v[24:25], v[26:27]\n\t"
    "v_pk_mul_f32 v[34:35], v[24:25], v[24:25]\n\t"
    "v_pk_add_f32 v[36:37], v[28:29], v[30:31]\n\t"
    "v_pk_fma_f32 v[34:35], v[26:27], v[26:27], v[34:35]\n\t"
    "v_pk_add_f32 v[32:33], v[32:33], v[36:37]\n\t"
    "v_pk_fma_f32 v[34:35], v[28:29], v[28:29], v[34:35]\n\t"
    "v_add_f32 v32, v32, v33\n\t"
    "v_pk_fma_f32 v[34:35], v[30:31], v[30:31], v[34:35]\n\t"
    "v_cndmask_b32 v32, 0, v32, s[72:73]\n\t"
    "v_add_f32 v34, v34, v35\n\t"
    "v_cndmask_b32 v34, 0, v34, s[72:73]\n\t"
    "s_nop 1\n\t"
    "v_permlane16_swap_b32 v32, v34\n\t"
    "v_add_f32 v32, v32, v34\n\t"
    "v_mov_b32 v33, v32\n\t"
    "s_nop 1\n\t"
    "v_permlane32_swap_b32 v32, v33\n\t"
    "v_add_f32 v32, v32, v33\n\t"
    "s_nop 1\n\t"
    "v_add_f32_dpp v32, v32, v32 quad_perm:[1,0,3,2] row_mask:0xf bank_mask:0xf\n\t"
    "s_nop 1\n\t"
    "v_add_f32_dpp v32, v32, v32 quad_perm:[2,3,0,1] row_mask:0xf bank_mask:0xf\n\t"
    "s_nop 1\n\t"
    "v_add_f32_dpp v32, v32, v32 row_half_mirror row_mask:0xf bank_mask:0xf\n\t"
    "s_nop 1\n\t"
    "v_add_f32_dpp v32, v32, v32 row_mirror row_mask:0xf bank_mask:0xf\n\t"
    "v_mul_f32 v32, 0x3b124925, v32\n\t"
    "s_nop 0\n\t"
    "v_readlane_b32 s74, v32, 0\n\t"
    "v_readlane_b32 s75, v32, 16\n\t"
    "v_mov_b32 v35, 0x3727c5ac\n\t"
    "v_mov_b32 v33, s74\n\t"
    "v_mov_b32 v34, s75\n\t"
    "v_fma_f32 v34, -v33, v33, v34\n\t"
    "v_max_f32 v34, 0, v34\n\t"
    "v_add_f32 v32, v34, v35\n\t"
    "v_rsq_f32 v32, v32\n\t"
    "s_nop 0\n\t"
    "v_mul_f32 v33, -v33, v32\n\t"
    "v_pk_fma_f32 v[24:25], v[24:25], v[32:33], v[32:33] op_sel:[0,0,1] op_sel_hi:[1,0,1]\n\t"
    "v_pk_fma_f32 v[26:27], v[26:27], v[32:33], v[32:33] op_sel:[0,0,1] op_sel_hi:[1,0,1]\n\t"
    "v_pk_fma_f32 v[28:29], v[28:29], v[32:33], v[32:33] op_sel:[0,0,1] op_sel_hi:[1,0,1]\n\t"
    "v_pk_fma_f32 v[30:31], v[30:31], v[32:33], v[32:33] op_sel:[0,0,1] op_sel_hi:[1,0,1]\n\t"
    "v_cvt_pk_bf16_f32 v16, v24, v25\n\t"
    "v_cvt_pk_bf16_f32 v17, v26, v27\n\t"
    "v_cvt_pk_bf16_f32 v18, v28, v29\n\t"
    "v_cvt_pk_bf16_f32 v19, v30, v31\n\t"
    "s_mov_b64 exec, s[72:73]\n\t"
    "ds_write_b128 v4, v[16:19] offset:0\n\t"
    "s_mov_b64 exec, -1\n\t"
    "s_mov_b64 exec, s[72:73]\n\t"
    "ds_write_b128 v4, v[12:15] offset:8064\n\t"
    "s_mov_b64 exec, -1\n\t"
    "v_lshlrev_b32 v16, 16, v12\n\t"
    "v_and_b32 v17, 0xffff0000, v12\n\t"
    "v_lshlrev_b32 v18, 16, v13\n\t"
    "v_and_b32 v19, 0xffff0000, v13\n\t"
    "v_lshlrev_b32 v20, 16, v14\n\t"
    "v_and_b32 v21, 0xffff0000, v14\n\t"
    "v_lshlrev_b32 v22, 16, v15\n\t"
    "v_and_b32 v23, 0xffff0000, v15\n\t"
    "s_nop 0\n\t"
    "v_readfirstlane_b32 s74, v16\n\t"
    "s_nop 1\n\t"
    "v_pk_add_f32 v[24:25], v[16:17], s[74:75] op_sel_hi:[1,0] neg_lo:[0,1] neg_hi:[0,1]\n\t"
    "v_pk_add_f32 v[26:27], v[18:19], s[74:75] op_sel_hi:[1,0] neg_lo:[0,1] neg_hi:[0,1]\n\t"
    "v_pk_add_f32 v[28:29], v[20:21], s[74:75] op_sel_hi:[1,0] neg_lo:[0,1] neg_hi:[0,1]\n\t"
    "v_pk_add_f32 v[30:31], v[22:23], s[74:75] op_sel_hi:[1,0] neg_lo:[0,1] neg_hi:[0,1]\n\t"
    "v_pk_add_f32 v[32:33], v[24:25], v[26:27]\n\t"
    "v_pk_mul_f32 v[34:35], v[24:25], v[24:25]\n\t"
    "v_pk_add_f32 v[36:37], v[28:29], v[30:31]\n\t"
    "v_pk_fma_f32 v[34:35], v[26:27], v[26:27], v[34:35]\n\t"
    "v_pk_add_f32 v[32:33], v[32:33], v[36:37]\n\t"
    "v_pk_fma_f32 v[34:35], v[28:29], v[28:29], v[34:35]\n\t"
    "v_add_f32 v32, v32, v33\n\t"
    "v_pk_fma_f32 v[34:35], v[30:31], v[30:31], v[34:35]\n\t"
    "v_cndmask_b32 v32, 0, v32, s[72:73]\n\t"
    "v_add_f32 v34, v34, v35\n\t"
    "v_cndmask_b32 v34, 0, v34, s[72:73]\n\t"
    "s_nop 1\n\t"
    "v_permlane16_swap_b32 v32, v34\n\t"
    "v_add_f32 v32, v32, v34\n\t"
    "v_mov_b32 v33, v32\n\t"
    "s_nop 1\n\t"
    "v_permlane32_swap_b32 v32, v33\n\t"
    "v_add_f32 v32, v32, v33\n\t"
    "s_nop 1\n\t"
    "v_add_f32_dpp v32, v32, v32 quad_perm:[1,0,3,2] row_mask:0xf bank_mask:0xf\n\t"
    "s_nop 1\n\t"
    "v_add_f32_dpp v32, v32, v32 quad_perm:[2,3,0,1] row_mask:0xf bank_mask:0xf\n\t"
    "s_nop 1\n\t"
    "v_add_f32_dpp v32, v32, v32 row_half_mirror row_mask:0xf bank_mask:0xf\n\t"
    "s_nop 1\n\t"
    "v_add_f32_dpp v32, v32, v32 row_mirror row_mask:0xf bank_mask:0xf\n\t"
    "v_mul_f32 v32, 0x3b124925, v32\n\t"
    "s_nop 0\n\t"
    "v_readlane_b32 s74, v32, 0\n\t"
    "v_readlane_b32 s75, v32, 16\n\t"
    "v_mov_b32 v35, 0x3727c5ac\n\t"
    "v_mov_b32 v33, s74\n\t"
    "v_mov_b32 v34, s75\n\t"
    "v_fma_f32 v34, -v33, v33, v34\n\t"
    "v_max_f32 v34, 0, v34\n\t"
    "v_add_f32 v32, v34, v35\n\t"
    "v_rsq_f32 v32, v32\n\t"
    "s_nop 0\n\t"
    "v_mul_f32 v33, -v33, v32\n\t"
    "v_pk_fma_f32 v[24:25], v[24:25], v[32:33], v[32:33] op_sel:[0,0,1] op_sel_hi:[1,0,1]\n\t"
    "v_pk_fma_f32 v[26:27], v[26:27], v[32:33], v[32:33] op_sel:[0,0,1] op_sel_hi:[1,0,1]\n\t"
    "v_pk_fma_f32 v[28:29], v[28:29], v[32:33], v[32:33] op_sel:[0,0,1] op_sel_hi:[1,0,1]\n\t"
    "v_pk_fma_f32 v[30:31], v[30:31], v[32:33], v[32:33] op_sel:[0,0,1] op_sel_hi:[1,0,1]\n\t"
    "v_cvt_pk_bf16_f32 v16, v24, v25\n\t"
    "v_cvt_pk_bf16_f32 v17, v26, v27\n\t"
    "v_cvt_pk_bf16_f32 v18, v28, v29\n\t"
    "v_cvt_pk_bf16_f32 v19, v30, v31\n\t"
    "s_mov_b64 exec, s[72:73]\n\t"
    "ds_write_b128 v4, v[16:19] offset:896\n\t"
    "s_mov_b64 exec, -1\n\t"
    "s_waitcnt vmcnt(12)\n\t"
    "ds_read_b64_tr_b16 a[0:1], v6 offset:0\n\t"
    "ds_read_b64_tr_b16 a[2:3], v6 offset:512\n\t"
    "ds_read_b64_tr_b16 a[4:5], v6 offset:8\n\t"
    "ds_read_b64_tr_b16 a[6:7], v6 offset:520\n\t"
    "ds_read_b64_tr_b16 a[8:9], v6 offset:16\n\t"
    "ds_read_b64_tr_b16 a[10:11], v6 offset:528\n\t"
    "ds_read_b64_tr_b16 a[12:13], v6 offset:24\n\t"
    "ds_read_b64_tr_b16 a[14:15], v6 offset:536\n\t"
    "s_waitcnt lgkmcnt(0)\n\t"
    "s_add_u32 m0, s61, 0\n\t"
    "s_nop 0\n\t"
    "buffer_load_dwordx4 v5, s[44:47], s60 offen nt lds\n\t"
    "s_add_u32 s60, s60, s65\n\t"
    "s_add_u32 m0, s61, 1024\n\t"
    "s_nop 0\n\t"
    "buffer_load_dwordx4 v5, s[44:47], s60 offen nt lds\n\t"
    "s_add_u32 s60, s60, s65\n\t"
    "s_add_u32 m0, s61, 2048\n\t"
    "s_nop 0\n\t"
    "buffer_load_dwordx4 v5, s[44:47], s60 offen nt lds\n\t"
    "s_add_u32 s60, s60, s65\n\t"
    "s_add_u32 m0, s61, 3072\n\t"
    "s_nop 0\n\t"
    "buffer_load_dwordx4 v5, s[44:47], s60 offen nt lds\n\t"
    "s_add_u32 s60, s60, s65\n\t"
    "s_waitcnt vmcnt(12)\n\t"
    "ds_read_b64_tr_b16 a[16:17], v6 offset:4096\n\t"
    "ds_read_b64_tr_b16 a[18:19], v6 offset:4608\n\t"
    "ds_read_b64_tr_b16 a[20:21], v6 offset:4104\n\t"
    "ds_read_b64_tr_b16 a[22:23], v6 offset:4616\n\t"
    "ds_read_b64_tr_b16 a[24:25], v6 offset:4112\n\t"
    "ds_read_b64_tr_b16 a[26:27], v6 offset:4624\n\t"
    "ds_read_b64_tr_b16 a[28:29], v6 offset:4120\n\t"
    "ds_read_b64_tr_b16 a[30:31], v6 offset:4632\n\t"
    "s_waitcnt lgkmcnt(0)\n\t"
    "s_add_u32 m0, s61, 4096\n\t"
    "s_nop 0\n\t"
    "buffer_load_dwordx4 v5, s[44:47], s60 offen nt lds\n\t"
    "s_add_u32 s60, s60, s65\n\t"
    "s_add_u32 m0, s61, 5120\n\t"
    "s_nop 0\n\t"
    "buffer_load_dwordx4 v5, s[44:47], s60 offen nt lds\n\t"
    "s_add_u32 s60, s60, s65\n\t"
    "s_add_u32 m0, s61, 6144\n\t"
    "s_nop 0\n\t"
    "buffer_load_dwordx4 v5, s[44:47], s60 offen nt lds\n\t"
    "s_add_u32 s60, s60, s65\n\t"
    "s_add_u32 m0, s61, 7168\n\t"
    "s_nop 0\n\t"
    "buffer_load_dwordx4 v5, s[44:47], s60 offen nt lds\n\t"
    "s_add_u32 s60, s60, s65\n\t"
    "s_waitcnt vmcnt(12)\n\t"
    "ds_read_b64_tr_b16 a[32:33], v6 offset:8192\n\t"
    "ds_read_b64_tr_b16 a[34:35], v6 offset:8704\n\t"
    "ds_read_b64_tr_b16 a[36:37], v6 offset:8200\n\t"
    "ds_read_b64_tr_b16 a[38:39], v6 offset:8712\n\t"
    "ds_read_b64_tr_b16 a[40:41], v6 offset:8208\n\t"
    "ds_read_b64_tr_b16 a[42:43], v6 offset:8720\n\t"
    "ds_read_b64_tr_b16 a[44:45], v6 offset:8216\n\t"
    "ds_read_b64_tr_b16 a[46:47], v6 offset:8728\n\t"
    "s_waitcnt lgkmcnt(0)\n\t"
    "s_add_u32 m0, s61, 8192\n\t"
    "s_nop 0\n\t"
    "buffer_load_dwordx4 v5, s[44:47], s60 offen nt lds\n\t"
    "s_add_u32 s60, s60, s65\n\t"
    "s_add_u32 m0, s61, 9216\n\t"
    "s_nop 0\n\t"
    "buffer_load_dwordx4 v5, s[44:47], s60 offen nt lds\n\t"
    "s_add_u32 s60, s60, s65\n\t"
    "s_add_u32 m0, s61, 10240\n\t"
    "s_nop 0\n\t"
    "buffer_load_dwordx4 v5, s[44:47], s60 offen nt lds\n\t"
    "s_add_u32 s60, s60, s65\n\t"
    "s_add_u32 m0, s61, 11264\n\t"
    "s_nop 0\n\t"
    "buffer_load_dwordx4 v5, s[44:47], s60 offen nt lds\n\t"
    "s_add_u32 s60, s60, s65\n\t"
    "s_waitcnt vmcnt(12)\n\t"
    "ds_read_b64_tr_b16 a[48:49], v6 offset:12288\n\t"
    "ds_read_b64_tr_b16 a[50:51], v6 offset:12800\n\t"
    "ds_read_b64_tr_b16 a[52:53], v6 offset:12296\n\t"
    "ds_read_b64_tr_b16 a[54:55], v6 offset:12808\n\t"
    "ds_read_b64_tr_b16 a[56:57], v6 offset:12304\n\t"
    "ds_read_b64_tr_b16 a[58:59], v6 offset:12816\n\t"
    "ds_read_b64_tr_b16 a[60:61], v6 offset:12312\n\t"
    "ds_read_b64_tr_b16 a[62:63], v6 offset:12824\n\t"
    "s_waitcnt lgkmcnt(0)\n\t"
    "s_add_u32 m0, s61, 12288\n\t"
    "s_nop 0\n\t"
    "buffer_load_dwordx4 v5, s[44:47], s60 offen nt lds\n\t"
    "s_add_u32 s60, s60, s65\n\t"
    "s_add_u32 m0, s61, 13312\n\t"
    "s_nop 0\n\t"
    "buffer_load_dwordx4 v5, s[44:47], s60 offen nt lds\n\t"
    "s_add_u32 s60, s60, s65\n\t"
    "s_add_u32 m0, s61, 14336\n\t"
    "s_nop 0\n\t"
    "buffer_load_dwordx4 v5, s[44:47], s60 offen nt lds\n\t"
    "s_add_u32 s60, s60, s65\n\t"
    "s_add_u32 m0, s61, 15360\n\t"
    "s_nop 0\n\t"
    "buffer_load_dwordx4 v5, s[44:47], s60 offen nt lds\n\t"
    "s_add_u32 s60, s60, s65\n\t"
    "s_waitcnt vmcnt(12)\n\t"
    "ds_read_b64_tr_b16 a[64:65], v6 offset:0\n\t"
    "ds_read_b64_tr_b16 a[66:67], v6 offset:512\n\t"
    "ds_read_b64_tr_b16 a[68:69], v6 offset:8\n\t"
    "ds_read_b64_tr_b16 a[70:71], v6 offset:520\n\t"
    "ds_read_b64_tr_b16 a[72:73], v6 offset:16\n\t"
    "ds_read_b64_tr_b16 a[74:75], v6 offset:528\n\t"
    "ds_read_b64_tr_b16 a[76:77], v6 offset:24\n\t"
    "ds_read_b64_tr_b16 a[78:79], v6 offset:536\n\t"
    "s_waitcnt lgkmcnt(0)\n\t"
    "s_add_u32 m0, s61, 0\n\t"
    "s_nop 0\n\t"
    "buffer_load_dwordx4 v5, s[44:47], s60 offen nt lds\n\t"
    "s_add_u32 s60, s60, s65\n\t"
    "s_add_u32 m0, s61, 1024\n\t"
    "s_nop 0\n\t"
    "buffer_load_dwordx4 v5, s[44:47], s60 offen nt lds\n\t"
    "s_add_u32 s60, s60, s65\n\t"
    "s_add_u32 m0, s61, 2048\n\t"
    "s_nop 0\n\t"
    "buffer_load_dwordx4 v5, s[44:47], s60 offen nt lds\n\t"
    "s_add_u32 s60, s60, s65\n\t"
    "s_add_u32 m0, s61, 3072\n\t"
    "s_nop 0\n\t"
    "buffer_load_dwordx4 v5, s[44:47], s60 offen nt lds\n\t"
    "s_add_u32 s60, s60, s65\n\t"
    "s_waitcnt vmcnt(12)\n\t"
    "ds_read_b64_tr_b16 a[80:81], v6 offset:4096\n\t"
    "ds_read_b64_tr_b16 a[82:83], v6 offset:4608\n\t"
    "ds_read_b64_tr_b16 a[84:85], v6 offset:4104\n\t"
    "ds_read_b64_tr_b16 a[86:87], v6 offset:4616\n\t"
    "ds_read_b64_tr_b16 a[88:89], v6 offset:4112\n\t"
    "ds_read_b64_tr_b16 a[90:91], v6 offset:4624\n\t"
    "ds_read_b64_tr_b16 a[92:93], v6 offset:4120\n\t"
    "ds_read_b64_tr_b16 a[94:95], v6 offset:4632\n\t"
    "s_waitcnt lgkmcnt(0)\n\t"
    "s_add_u32 m0, s61, 4096\n\t"
    "s_nop 0\n\t"
    "buffer_load_dwordx4 v5, s[44:47], s60 offen nt lds\n\t"
    "s_add_u32 s60, s60, s65\n\t"
    "s_add_u32 m0, s61, 5120\n\t"
    "s_nop 0\n\t"
    "buffer_load_dwordx4 v5, s[44:47], s60 offen nt lds\n\t"
    "s_add_u32 s60, s60, s65\n\t"
    "s_add_u32 m0, s61, 6144\n\t"
    "s_nop 0\n\t"
    "buffer_load_dwordx4 v5, s[44:47], s60 offen nt lds\n\t"
    "s_add_u32 s60, s60, s65\n\t"
    "s_add_u32 m0, s61, 7168\n\t"
    "s_nop 0\n\t"
    "buffer_load_dwordx4 v5, s[44:47], s60 offen nt lds\n\t"
    "s_add_u32 s60, s60, s65\n\t"
    "s_waitcnt vmcnt(12)\n\t"
    "ds_read_b64_tr_b16 a[96:97], v6 offset:8192\n\t"
    "ds_read_b64_tr_b16 a[98:99], v6 offset:8704\n\t"
    "ds_read_b64_tr_b16 a[100:101], v6 offset:8200\n\t"
    "ds_read_b64_tr_b16 a[102:103], v6 offset:8712\n\t"
    "ds_read_b64_tr_b16 a[104:105], v6 offset:8208\n\t"
    "ds_read_b64_tr_b16 a[106:107], v6 offset:8720\n\t"
    "ds_read_b64_tr_b16 a[108:109], v6 offset:8216\n\t"
    "ds_read_b64_tr_b16 a[110:111], v6 offset:8728\n\t"
    "s_waitcnt lgkmcnt(0)\n\t"
    "s_add_u32 m0, s61, 8192\n\t"
    "s_nop 0\n\t"
    "buffer_load_dwordx4 v5, s[44:47], s60 offen nt lds\n\t"
    "s_add_u32 s60, s60, s65\n\t"
    "s_add_u32 m0, s61, 9216\n\t"
    "s_nop 0\n\t"
    "buffer_load_dwordx4 v5, s[44:47], s60 offen nt lds\n\t"
    "s_add_u32 s60, s60, s65\n\t"
    "s_add_u32 m0, s61, 10240\n\t"
    "s_nop 0\n\t"
    "buffer_load_dwordx4 v5, s[44:47], s60 offen nt lds\n\t"
    "s_add_u32 s60, s60, s65\n\t"
    "s_add_u32 m0, s61, 11264\n\t"
    "s_nop 0\n\t"
    "buffer_load_dwordx4 v5, s[44:47], s60 offen nt lds\n\t"
    "s_add_u32 s60, s60, s65\n\t"
    "s_waitcnt vmcnt(12)\n\t"
    "ds_read_b64_tr_b16 a[112:113], v6 offset:12288\n\t"
    "ds_read_b64_tr_b16 a[114:115], v6 offset:12800\n\t"
    "ds_read_b64_tr_b16 a[116:117], v6 offset:12296\n\t"
    "ds_read_b64_tr_b16 a[118:119], v6 offset:12808\n\t"
    "ds_read_b64_tr_b16 a[120:121], v6 offset:12304\n\t"
    "ds_read_b64_tr_b16 a[122:123], v6 offset:12816\n\t"
    "ds_read_b64_tr_b16 a[124:125], v6 offset:12312\n\t"
    "ds_read_b64_tr_b16 a[126:127], v6 offset:12824\n\t"
    "s_waitcnt lgkmcnt(0)\n\t"
    "s_add_u32 m0, s61, 12288\n\t"
    "s_nop 0\n\t"
    "buffer_load_dwordx4 v5, s[44:47], s60 offen nt lds\n\t"
    "s_add_u32 s60, s60, s65\n\t"
    "s_add_u32 m0, s61, 13312\n\t"
    "s_nop 0\n\t"
    "buffer_load_dwordx4 v5, s[44:47], s60 offen nt lds\n\t"
    "s_add_u32 s60, s60, s65\n\t"
    "s_add_u32 m0, s61, 14336\n\t"
    "s_nop 0\n\t"
    "buffer_load_dwordx4 v5, s[44:47], s60 offen nt lds\n\t"
    "s_add_u32 s60, s60, s65\n\t"
    "s_add_u32 m0, s61, 15360\n\t"
    "s_nop 0\n\t"
    "buffer_load_dwordx4 v5, s[44:47], s60 offen nt lds\n\t"
    "s_add_u32 s60, s60, s65\n\t"
    "s_waitcnt vmcnt(12)\n\t"
    "ds_read_b64_tr_b16 a[128:129], v6 offset:0\n\t"
    "ds_read_b64_tr_b16 a[130:131], v6 offset:512\n\t"
    "ds_read_b64_tr_b16 a[132:133], v6 offset:8\n\t"
    "ds_read_b64_tr_b16 a[134:135], v6 offset:520\n\t"
    "ds_read_b64_tr_b16 a[136:137], v6 offset:16\n\t"
    "ds_read_b64_tr_b16 a[138:139], v6 offset:528\n\t"
    "ds_read_b64_tr_b16 a[140:141], v6 offset:24\n\t"
    "ds_read_b64_tr_b16 a[142:143], v6 offset:536\n\t"
    "s_waitcnt lgkmcnt(0)\n\t"
    "s_add_u32 m0, s61, 0\n\t"
    "s_nop 0\n\t"
    "buffer_load_dwordx4 v5, s[44:47], s60 offen nt lds\n\t"
    "s_add_u32 s60, s60, s65\n\t"
    "s_add_u32 m0, s61, 1024\n\t"
    "s_nop 0\n\t"
    "buffer_load_dwordx4 v5, s[44:47], s60 offen nt lds\n\t"
    "s_add_u32 s60, s60, s65\n\t"
    "s_add_u32 m0, s61, 2048\n\t"
    "s_nop 0\n\t"
    "buffer_load_dwordx4 v5, s[44:47], s60 offen nt lds\n\t"
    "s_add_u32 s60, s60, s65\n\t"
    "s_add_u32 m0, s61, 3072\n\t"
    "s_nop 0\n\t"
    "buffer_load_dwordx4 v5, s[44:47], s60 offen nt lds\n\t"
    "s_add_u32 s60, s60, s65\n\t"
    "s_waitcnt vmcnt(12)\n\t"
    "ds_read_b64_tr_b16 a[144:145], v6 offset:4096\n\t"
    "ds_read_b64_tr_b16 a[146:147], v6 offset:4608\n\t"
    "ds_read_b64_tr_b16 a[148:149], v6 offset:4104\n\t"
    "ds_read_b64_tr_b16 a[150:151], v6 offset:4616\n\t"
    "ds_read_b64_tr_b16 a[152:153], v6 offset:4112\n\t"
    "ds_read_b64_tr_b16 a[154:155], v6 offset:4624\n\t"
    "ds_read_b64_tr_b16 a[156:157], v6 offset:4120\n\t"
    "ds_read_b64_tr_b16 a[158:159], v6 offset:4632\n\t"
    "s_waitcnt lgkmcnt(0)\n\t"
    "s_add_u32 m0, s61, 4096\n\t"
    "s_nop 0\n\t"
    "buffer_load_dwordx4 v5, s[44:47], s60 offen nt lds\n\t"
    "s_add_u32 s60, s60, s65\n\t"
    "s_add_u32 m0, s61, 5120\n\t"
    "s_nop 0\n\t"
    "buffer_load_dwordx4 v5, s[44:47], s60 offen nt lds\n\t"
    "s_add_u32 s60, s60, s65\n\t"
    "s_add_u32 m0, s61, 6144\n\t"
    "s_nop 0\n\t"
    "buffer_load_dwordx4 v5, s[44:47], s60 offen nt lds\n\t"
    "s_add_u32 s60, s60, s65\n\t"
    "s_add_u32 m0, s61, 7168\n\t"
    "s_nop 0\n\t"
    "buffer_load_dwordx4 v5, s[44:47], s60 offen nt lds\n\t"
    "s_add_u32 s60, s60, s65\n\t"
    "s_waitcnt vmcnt(12)\n\t"
    "ds_read_b64_tr_b16 a[160:161], v6 offset:8192\n\t"
    "ds_read_b64_tr_b16 a[162:163], v6 offset:8704\n\t"
    "ds_read_b64_tr_b16 a[164:165], v6 offset:8200\n\t"
    "ds_read_b64_tr_b16 a[166:167], v6 offset:8712\n\t"
    "ds_read_b64_tr_b16 a[168:169], v6 offset:8208\n\t"
    "ds_read_b64_tr_b16 a[170:171], v6 offset:8720\n\t"
    "ds_read_b64_tr_b16 a[172:173], v6 offset:8216\n\t"
    "ds_read_b64_tr_b16 a[174:175], v6 offset:8728\n\t"
    "s_waitcnt vmcnt(8)\n\t"
    "ds_read_b64_tr_b16 a[176:177], v6 offset:12288\n\t"
    "ds_read_b64_tr_b16 a[178:179], v6 offset:12800\n\t"
    "ds_read_b64_tr_b16 a[180:181], v6 offset:12296\n\t"
    "ds_read_b64_tr_b16 a[182:183], v6 offset:12808\n\t"
    "ds_read_b64_tr_b16 a[184:185], v6 offset:12304\n\t"
    "ds_read_b64_tr_b16 a[186:187], v6 offset:12816\n\t"
    "ds_read_b64_tr_b16 a[188:189], v6 offset:12312\n\t"
    "ds_read_b64_tr_b16 a[190:191], v6 offset:12824\n\t"
    "s_waitcnt vmcnt(4)\n\t"
    "ds_read_b64_tr_b16 a[192:193], v6 offset:0\n\t"
    "ds_read_b64_tr_b16 a[194:195], v6 offset:512\n\t"
    "ds_read_b64_tr_b16 a[196:197], v6 offset:8\n\t"
    "ds_read_b64_tr_b16 a[198:199], v6 offset:520\n\t"
    "ds_read_b64_tr_b16 a[200:201], v6 offset:16\n\t"
    "ds_read_b64_tr_b16 a[202:203], v6 offset:528\n\t"
    "ds_read_b64_tr_b16 a[204:205], v6 offset:24\n\t"
    "ds_read_b64_tr_b16 a[206:207], v6 offset:536\n\t"
    "s_waitcnt vmcnt(0)\n\t"
    "ds_read_b64_tr_b16 a[208:209], v6 offset:4096\n\t"
    "ds_read_b64_tr_b16 a[210:211], v6 offset:4608\n\t"
    "ds_read_b64_tr_b16 a[212:213], v6 offset:4104\n\t"
    "ds_read_b64_tr_b16 a[214:215], v6 offset:4616\n\t"
    "ds_read_b64_tr_b16 a[216:217], v6 offset:4112\n\t"
    "ds_read_b64_tr_b16 a[218:219], v6 offset:4624\n\t"
    "ds_read_b64_tr_b16 a[220:221], v6 offset:4120\n\t"
    "ds_read_b64_tr_b16 a[222:223], v6 offset:4632\n\t"
    "s_waitcnt lgkmcnt(0)\n\t"
    "s_barrier\n\t"
    "s_mov_b64 s[76:77], %3\n\t"
    "s_and_b32 s77, s77, 0xffff\n\t"
    "s_mov_b32 s78, 0x380000\n\t"
    "s_mov_b32 s79, 0x20000\n\t"
    "s_mov_b64 s[44:45], %4\n\t"
    "s_and_b32 s45, s45, 0xffff\n\t"
    "s_mov_b32 s46, 0x10000000\n\t"
    "s_mov_b32 s47, 0x20000\n\t"
    "s_mov_b64 s[48:49], %5\n\t"
    "s_and_b32 s49, s49, 0xffff\n\t"
    "s_mov_b32 s50, 0x10000000\n\t"
    "s_mov_b32 s51, 0x20000\n\t"
    "v_lshlrev_b32 v5, 4, v0\n\t"
    "s_mul_i32 s80, s53, 229376\n\t"
    "s_mul_i32 s64, s52, 7168\n\t"
    "s_add_u32 s80, s80, s64\n\t"
    "s_add_u32 s81, s64, 29696\n\t"
    "s_add_u32 m0, s81, 0\n\t"
    "s_nop 0\n\t"
    "buffer_load_dwordx4 v5, s[76:79], s80 offen offset:0 nt lds\n\t"
    "buffer_load_dwordx4 v5, s[76:79], s80 offen offset:1024 nt lds\n\t"
    "buffer_load_dwordx4 v5, s[76:79], s80 offen offset:2048 nt lds\n\t"
    "buffer_load_dwordx4 v5, s[76:79], s80 offen offset:3072 nt lds\n\t"
    "s_add_u32 m0, m0, 4096\n\t"
    "s_add_u32 s64, s80, 4096\n\t"
    "buffer_load_dwordx4 v5, s[76:79], s64 offen offset:0 nt lds\n\t"
    "buffer_load_dwordx4 v5, s[76:79], s64 offen offset:1024 nt lds\n\t"
    "buffer_load_dwordx4 v5, s[76:79], s64 offen offset:2048 nt lds\n\t"
    "s_add_u32 s80, s80, 28672\n\t"
    "s_add_u32 m0, s81, 28672\n\t"
    "s_nop 0\n\t"
    "buffer_load_dwordx4 v5, s[76:79], s80 offen offset:0 nt lds\n\t"
    "buffer_load_dwordx4 v5, s[76:79], s80 offen offset:1024 nt lds\n\t"
    "buffer_load_dwordx4 v5, s[76:79], s80 offen offset:2048 nt lds\n\t"
    "buffer_load_dwordx4 v5, s[76:79], s80 offen offset:3072 nt lds\n\t"
    "s_add_u32 m0, m0, 4096\n\t"
    "s_add_u32 s64, s80, 4096\n\t"
    "buffer_load_dwordx4 v5, s[76:79], s64 offen offset:0 nt lds\n\t"
    "buffer_load_dwordx4 v5, s[76:79], s64 offen offset:1024 nt lds\n\t"
    "buffer_load_dwordx4 v5, s[76:79], s64 offen offset:2048 nt lds\n\t"
    "s_add_u32 s80, s80, 28672\n\t"
    "v_and_b32 v7, 15, v0\n\t"
    "v_lshrrev_b32 v4, 4, v0\n\t"
    "v_and_b32 v1, 7, v0\n\t"
    "v_mul_u32_u24 v1, 896, v1\n\t"
    "v_lshl_add_u32 v1, v4, 4, v1\n\t"
    "v_lshlrev_b32 v3, 9, v7\n\t"
    "v_lshl_add_u32 v3, v4, 5, v3\n\t"
    "s_lshl_b32 s64, s52, 7\n\t"
    "s_add_u32 s37, s64, 24576\n\t"
    "v_add_u32 v3, s37, v3\n\t"
    "s_lshl_b32 s97, s53, 9\n\t"
    "s_add_u32 s97, s97, s64\n\t"
    "s_mov_b32 s94, 0x00ff00ff\n\t"
    "s_mov_b32 s95, 0x00ff00ff\n\t"
    "s_mov_b64 s[92:93], -1\n\t"
    "s_call_b64 s[68:69], .Lc_q_%=\n\t"
    "v_and_b32 v4, 15, v0\n\t"
    "v_lshlrev_b32 v4, 3, v4\n\t"
    "s_mov_b32 s64, 3137792\n\t"
    "buffer_load_dwordx2 v[8:9], v4, s[40:43], s64 offen sc1\n\t"
    "v_lshlrev_b32 v5, 4, v0\n\t"
    "v_lshrrev_b32 v6, 4, v0\n\t"
    "v_lshlrev_b32 v6, 3, v6\n\t"
    "v_bfe_u32 v7, v0, 2, 2\n\t"
    "v_add_u32 v6, v6, v7\n\t"
    "v_mul_u32_u24 v6, 896, v6\n\t"
    "v_and_b32 v7, 3, v0\n\t"
    "v_lshl_add_u32 v6, v7, 3, v6\n\t"
    "s_mul_i32 s63, s52, 224\n\t"
    "s_add_u32 s63, s63, 29696\n\t"
    "v_add_u32 v6, s63, v6\n\t"
    "v_add_u32 v7, 57344, v6\n\t"
    "s_waitcnt vmcnt(7) lgkmcnt(0)\n\t"
    "s_barrier\n\t"
    "s_add_u32 m0, s81, 57344\n\t"
    "s_nop 0\n\t"
    "buffer_load_dwordx4 v5, s[76:79], s80 offen offset:0 nt lds\n\t"
    "buffer_load_dwordx4 v5, s[76:79], s80 offen offset:1024 nt lds\n\t"
    "buffer_load_dwordx4 v5, s[76:79], s80 offen offset:2048 nt lds\n\t"
    "buffer_load_dwordx4 v5, s[76:79], s80 offen offset:3072 nt lds\n\t"
    "s_add_u32 m0, m0, 4096\n\t"
    "s_add_u32 s64, s80, 4096\n\t"
    "buffer_load_dwordx4 v5, s[76:79], s64 offen offset:0 nt lds\n\t"
    "buffer_load_dwordx4 v5, s[76:79], s64 offen offset:1024 nt lds\n\t"
    "buffer_load_dwordx4 v5, s[76:79], s64 offen offset:2048 nt lds\n\t"
    "s_add_u32 s80, s80, 28672\n\t"
    "ds_read_b64_tr_b16 a[224:225], v6 offset:0\n\t"
    "ds_read_b64_tr_b16 a[226:227], v6 offset:3584\n\t"
    "ds_read_b64_tr_b16 a[228:229], v6 offset:32\n\t"
    "ds_read_b64_tr_b16 a[230:231], v6 offset:3616\n\t"
    "ds_read_b64_tr_b16 a[232:233], v6 offset:64\n\t"
    "ds_read_b64_tr_b16 a[234:235], v6 offset:3648\n\t"
    "ds_read_b64_tr_b16 a[236:237], v6 offset:96\n\t"
    "ds_read_b64_tr_b16 a[238:239], v6 offset:3680\n\t"
    "ds_read_b64_tr_b16 a[240:241], v6 offset:128\n\t"
    "ds_read_b64_tr_b16 a[242:243], v6 offset:3712\n\t"
    "ds_read_b64_tr_b16 a[244:245], v6 offset:160\n\t"
    "ds_read_b64_tr_b16 a[246:247], v6 offset:3744\n\t"
    "ds_read_b64_tr_b16 a[248:249], v6 offset:192\n\t"
    "ds_read_b64_tr_b16 a[250:251], v6 offset:3776\n\t"
    "s_waitcnt vmcnt(7) lgkmcnt(0)\n\t"
    "s_barrier\n\t"
    "s_add_u32 m0, s81, 0\n\t"
    "s_nop 0\n\t"
    "buffer_load_dwordx4 v5, s[76:79], s80 offen offset:0 nt lds\n\t"
    "buffer_load_dwordx4 v5, s[76:79], s80 offen offset:1024 nt lds\n\t"
    "buffer_load_dwordx4 v5, s[76:79], s80 offen offset:2048 nt lds\n\t"
    "buffer_load_dwordx4 v5, s[76:79], s80 offen offset:3072 nt lds\n\t"
    "s_add_u32 m0, m0, 4096\n\t"
    "s_add_u32 s64, s80, 4096\n\t"
    "buffer_load_dwordx4 v5, s[76:79], s64 offen offset:0 nt lds\n\t"
    "buffer_load_dwordx4 v5, s[76:79], s64 offen offset:1024 nt lds\n\t"
    "buffer_load_dwordx4 v5, s[76:79], s64 offen offset:2048 nt lds\n\t"
    "s_add_u32 s80, s80, 28672\n\t"
    "ds_read_b64_tr_b16 a[252:253], v6 offset:28672\n\t"
    "ds_read_b64_tr_b16 a[254:255], v6 offset:32256\n\t"
    "ds_read_b64_tr_b16 v[64:65], v6 offset:28704\n\t"
    "ds_read_b64_tr_b16 v[66:67], v6 offset:32288\n\t"
    "ds_read_b64_tr_b16 v[68:69], v6 offset:28736\n\t"
    "ds_read_b64_tr_b16 v[70:71], v6 offset:32320\n\t"
    "ds_read_b64_tr_b16 v[72:73], v6 offset:28768\n\t"
    "ds_read_b64_tr_b16 v[74:75], v6 offset:32352\n\t"
    "ds_read_b64_tr_b16 v[76:77], v6 offset:28800\n\t"
    "ds_read_b64_tr_b16 v[78:79], v6 offset:32384\n\t"
    "ds_read_b64_tr_b16 v[80:81], v6 offset:28832\n\t"
    "ds_read_b64_tr_b16 v[82:83], v6 offset:32416\n\t"
    "ds_read_b64_tr_b16 v[84:85], v6 offset:28864\n\t"
    "ds_read_b64_tr_b16 v[86:87], v6 offset:32448\n\t"
    "s_waitcnt vmcnt(7) lgkmcnt(0)\n\t"
    "s_barrier\n\t"
    "s_add_u32 m0, s81, 28672\n\t"
    "s_nop 0\n\t"
    "buffer_load_dwordx4 v5, s[76:79], s80 offen offset:0 nt lds\n\t"
    "buffer_load_dwordx4 v5, s[76:79], s80 offen offset:1024 nt lds\n\t"
    "buffer_load_dwordx4 v5, s[76:79], s80 offen offset:2048 nt lds\n\t"
    "buffer_load_dwordx4 v5, s[76:79], s80 offen offset:3072 nt lds\n\t"
    "s_add_u32 m0, m0, 4096\n\t"
    "s_add_u32 s64, s80, 4096\n\t"
    "buffer_load_dwordx4 v5, s[76:79], s64 offen offset:0 nt lds\n\t"
    "buffer_load_dwordx4 v5, s[76:79], s64 offen offset:1024 nt lds\n\t"
    "buffer_load_dwordx4 v5, s[76:79], s64 offen offset:2048 nt lds\n\t"
    "s_add_u32 s80, s80, 28672\n\t"
    "ds_read_b64_tr_b16 v[88:89], v7 offset:0\n\t"
    "ds_read_b64_tr_b16 v[90:91], v7 offset:3584\n\t"
    "ds_read_b64_tr_b16 v[92:93], v7 offset:32\n\t"
    "ds_read_b64_tr_b16 v[94:95], v7 offset:3616\n\t"
    "ds_read_b64_tr_b16 v[96:97], v7 offset:64\n\t"
    "ds_read_b64_tr_b16 v[98:99], v7 offset:3648\n\t"
    "ds_read_b64_tr_b16 v[100:101], v7 offset:96\n\t"
    "ds_read_b64_tr_b16 v[102:103], v7 offset:3680\n\t"
    "ds_read_b64_tr_b16 v[104:105], v7 offset:128\n\t"
    "ds_read_b64_tr_b16 v[106:107], v7 offset:3712\n\t"
    "ds_read_b64_tr_b16 v[108:109], v7 offset:160\n\t"
    "ds_read_b64_tr_b16 v[110:111], v7 offset:3744\n\t"
    "ds_read_b64_tr_b16 v[112:113], v7 offset:192\n\t"
    "ds_read_b64_tr_b16 v[114:115], v7 offset:3776\n\t"
    "s_waitcnt vmcnt(7) lgkmcnt(0)\n\t"
    "s_barrier\n\t"
    "s_add_u32 m0, s81, 57344\n\t"
    "s_nop 0\n\t"
    "buffer_load_dwordx4 v5, s[76:79], s80 offen offset:0 nt lds\n\t"
    "buffer_load_dwordx4 v5, s[76:79], s80 offen offset:1024 nt lds\n\t"
    "buffer_load_dwordx4 v5, s[76:79], s80 offen offset:2048 nt lds\n\t"
    "buffer_load_dwordx4 v5, s[76:79], s80 offen offset:3072 nt lds\n\t"
    "s_add_u32 m0, m0, 4096\n\t"
    "s_add_u32 s64, s80, 4096\n\t"
    "buffer_load_dwordx4 v5, s[76:79], s64 offen offset:0 nt lds\n\t"
    "buffer_load_dwordx4 v5, s[76:79], s64 offen offset:1024 nt lds\n\t"
    "buffer_load_dwordx4 v5, s[76:79], s64 offen offset:2048 nt lds\n\t"
    "s_add_u32 s80, s80, 28672\n\t"
    "ds_read_b64_tr_b16 v[116:117], v6 offset:0\n\t"
    "ds_read_b64_tr_b16 v[118:119], v6 offset:3584\n\t"
    "ds_read_b64_tr_b16 v[120:121], v6 offset:32\n\t"
    "ds_read_b64_tr_b16 v[122:123], v6 offset:3616\n\t"
    "ds_read_b64_tr_b16 v[124:125], v6 offset:64\n\t"
    "ds_read_b64_tr_b16 v[126:127], v6 offset:3648\n\t"
    "ds_read_b64_tr_b16 v[128:129], v6 offset:96\n\t"
    "ds_read_b64_tr_b16 v[130:131], v6 offset:3680\n\t"
    "ds_read_b64_tr_b16 v[132:133], v6 offset:128\n\t"
    "ds_read_b64_tr_b16 v[134:135], v6 offset:3712\n\t"
    "ds_read_b64_tr_b16 v[136:137], v6 offset:160\n\t"
    "ds_read_b64_tr_b16 v[138:139], v6 offset:3744\n\t"
    "ds_read_b64_tr_b16 v[140:141], v6 offset:192\n\t"
    "ds_read_b64_tr_b16 v[142:143], v6 offset:3776\n\t"
    "s_waitcnt vmcnt(7) lgkmcnt(0)\n\t"
    "s_barrier\n\t"
    "s_add_u32 m0, s81, 0\n\t"
    "s_nop 0\n\t"
    "buffer_load_dwordx4 v5, s[76:79], s80 offen offset:0 nt lds\n\t"
    "buffer_load_dwordx4 v5, s[76:79], s80 offen offset:1024 nt lds\n\t"
    "buffer_load_dwordx4 v5, s[76:79], s80 offen offset:2048 nt lds\n\t"
    "buffer_load_dwordx4 v5, s[76:79], s80 offen offset:3072 nt lds\n\t"
    "s_add_u32 m0, m0, 4096\n\t"
    "s_add_u32 s64, s80, 4096\n\t"
    "buffer_load_dwordx4 v5, s[76:79], s64 offen offset:0 nt lds\n\t"
    "buffer_load_dwordx4 v5, s[76:79], s64 offen offset:1024 nt lds\n\t"
    "buffer_load_dwordx4 v5, s[76:79], s64 offen offset:2048 nt lds\n\t"
    "s_add_u32 s80, s80, 28672\n\t"
    "ds_read_b64_tr_b16 v[144:145], v6 offset:28672\n\t"
    "ds_read_b64_tr_b16 v[146:147], v6 offset:32256\n\t"
    "ds_read_b64_tr_b16 v[148:149], v6 offset:28704\n\t"
    "ds_read_b64_tr_b16 v[150:151], v6 offset:32288\n\t"
    "ds_read_b64_tr_b16 v[152:153], v6 offset:28736\n\t"
    "ds_read_b64_tr_b16 v[154:155], v6 offset:32320\n\t"
    "ds_read_b64_tr_b16 v[156:157], v6 offset:28768\n\t"
    "ds_read_b64_tr_b16 v[158:159], v6 offset:32352\n\t"
    "ds_read_b64_tr_b16 v[160:161], v6 offset:28800\n\t"
    "ds_read_b64_tr_b16 v[162:163], v6 offset:32384\n\t"
    "ds_read_b64_tr_b16 v[164:165], v6 offset:28832\n\t"
    "ds_read_b64_tr_b16 v[166:167], v6 offset:32416\n\t"
    "ds_read_b64_tr_b16 v[168:169], v6 offset:28864\n\t"
    "ds_read_b64_tr_b16 v[170:171], v6 offset:32448\n\t"
    "s_waitcnt vmcnt(7) lgkmcnt(0)\n\t"
    "s_barrier\n\t"
    "s_add_u32 m0, s81, 28672\n\t"
    "s_nop 0\n\t"
    "buffer_load_dwordx4 v5, s[76:79], s80 offen offset:0 nt lds\n\t"
    "buffer_load_dwordx4 v5, s[76:79], s80 offen offset:1024 nt lds\n\t"
    "buffer_load_dwordx4 v5, s[76:79], s80 offen offset:2048 nt lds\n\t"
    "buffer_load_dwordx4 v5, s[76:79], s80 offen offset:3072 nt lds\n\t"
    "s_add_u32 m0, m0, 4096\n\t"
    "s_add_u32 s64, s80, 4096\n\t"
    "buffer_load_dwordx4 v5, s[76:79], s64 offen offset:0 nt lds\n\t"
    "buffer_load_dwordx4 v5, s[76:79], s64 offen offset:1024 nt lds\n\t"
    "buffer_load_dwordx4 v5, s[76:79], s64 offen offset:2048 nt lds\n\t"
    "s_add_u32 s80, s80, 28672\n\t"
    "ds_read_b64_tr_b16 v[172:173], v7 offset:0\n\t"
    "ds_read_b64_tr_b16 v[174:175], v7 offset:3584\n\t"
    "ds_read_b64_tr_b16 v[176:177], v7 offset:32\n\t"
    "ds_read_b64_tr_b16 v[178:179], v7 offset:3616\n\t"
    "ds_read_b64_tr_b16 v[180:181], v7 offset:64\n\t"
    "ds_read_b64_tr_b16 v[182:183], v7 offset:3648\n\t"
    "ds_read_b64_tr_b16 v[184:185], v7 offset:96\n\t"
    "ds_read_b64_tr_b16 v[186:187], v7 offset:3680\n\t"
    "ds_read_b64_tr_b16 v[188:189], v7 offset:128\n\t"
    "ds_read_b64_tr_b16 v[190:191], v7 offset:3712\n\t"
    "ds_read_b64_tr_b16 v[192:193], v7 offset:160\n\t"
    "ds_read_b64_tr_b16 v[194:195], v7 offset:3744\n\t"
    "ds_read_b64_tr_b16 v[196:197], v7 offset:192\n\t"
    "ds_read_b64_tr_b16 v[198:199], v7 offset:3776\n\t"
    "s_waitcnt vmcnt(7) lgkmcnt(0)\n\t"
    "s_barrier\n\t"
    "s_mov_b32 s54, -2\n\t"
    "s_call_b64 s[68:69], .Lc_xdma_%=\n\t"
    "ds_read_b64_tr_b16 v[200:201], v6 offset:0\n\t"
    "ds_read_b64_tr_b16 v[202:203], v6 offset:3584\n\t"
    "ds_read_b64_tr_b16 v[204:205], v6 offset:32\n\t"
    "ds_read_b64_tr_b16 v[206:207], v6 offset:3616\n\t"
    "ds_read_b64_tr_b16 v[208:209], v6 offset:64\n\t"
    "ds_read_b64_tr_b16 v[210:211], v6 offset:3648\n\t"
    "ds_read_b64_tr_b16 v[212:213], v6 offset:96\n\t"
    "ds_read_b64_tr_b16 v[214:215], v6 offset:3680\n\t"
    "ds_read_b64_tr_b16 v[216:217], v6 offset:128\n\t"
    "ds_read_b64_tr_b16 v[218:219], v6 offset:3712\n\t"
    "ds_read_b64_tr_b16 v[220:221], v6 offset:160\n\t"
    "ds_read_b64_tr_b16 v[222:223], v6 offset:3744\n\t"
    "ds_read_b64_tr_b16 v[224:225], v6 offset:192\n\t"
    "ds_read_b64_tr_b16 v[226:227], v6 offset:3776\n\t"
    "s_waitcnt vmcnt(8) lgkmcnt(0)\n\t"
    "s_barrier\n\t"
    "s_mov_b32 s54, -1\n\t"
    "s_call_b64 s[68:69], .Lc_xdma_%=\n\t"
    "ds_read_b64_tr_b16 v[228:229], v6 offset:28672\n\t"
    "ds_read_b64_tr_b16 v[230:231], v6 offset:32256\n\t"
    "ds_read_b64_tr_b16 v[232:233], v6 offset:28704\n\t"
    "ds_read_b64_tr_b16 v[234:235], v6 offset:32288\n\t"
    "ds_read_b64_tr_b16 v[236:237], v6 offset:28736\n\t"
    "ds_read_b64_tr_b16 v[238:239], v6 offset:32320\n\t"
    "ds_read_b64_tr_b16 v[240:241], v6 offset:28768\n\t"
    "ds_read_b64_tr_b16 v[242:243], v6 offset:32352\n\t"
    "ds_read_b64_tr_b16 v[244:245], v6 offset:28800\n\t"
    "ds_read_b64_tr_b16 v[246:247], v6 offset:32384\n\t"
    "ds_read_b64_tr_b16 v[248:249], v6 offset:28832\n\t"
    "ds_read_b64_tr_b16 v[250:251], v6 offset:32416\n\t"
    "ds_read_b64_tr_b16 v[252:253], v6 offset:28864\n\t"
    "ds_read_b64_tr_b16 v[254:255], v6 offset:32448\n\t"
    ".Lc_f2_%=:\n\t"
    "v_cmp_ne_u64 vcc, s[34:35], v[8:9]\n\t"
    "s_cbranch_vccz .Lc_f4_%=\n\t"
    "s_mov_b32 s64, 3137792\n\t"
    "buffer_load_dwordx2 v[8:9], v4, s[40:43], s64 offen sc1\n\t"
    "s_waitcnt vmcnt(0)\n\t"
    "s_add_u32 s58, s58, 1\n\t"
    "s_cmp_gt_u32 s58, 0x20000\n\t"
    "s_cbranch_scc1 .Lc_f4_%=\n\t"
    "s_sleep 2\n\t"
    "s_branch .Lc_f2_%=\n\t"
    ".Lc_f4_%=:\n\t"
    "s_waitcnt vmcnt(8) lgkmcnt(0)\n\t"
    "s_barrier\n\t"
    "s_cmp_eq_u32 s52, 0\n\t"
    "s_cselect_b64 s[76:77], %6, %7\n\t"
    "s_cmp_lt_u32 s52, 2\n\t"
    "s_cselect_b64 s[76:77], s[76:77], %8\n\t"
    "s_and_b32 s77, s77, 0xffff\n\t"
    "s_mov_b32 s78, 3584\n\t"
    "s_mov_b32 s79, 0x20000\n\t"
    "s_min_u32 s63, s52, 2\n\t"
    "s_lshl_b32 s63, s63, 12\n\t"
    "s_add_u32 m0, s63, 52736\n\t"
    "s_nop 0\n\t"
    "buffer_load_dwordx4 v5, s[76:79], 0 offen offset:0 lds\n\t"
    "buffer_load_dwordx4 v5, s[76:79], 0 offen offset:1024 lds\n\t"
    "buffer_load_dwordx4 v5, s[76:79], 0 offen offset:2048 lds\n\t"
    "buffer_load_dwordx4 v5, s[76:79], 0 offen offset:3072 lds\n\t"
    "s_call_b64 s[68:69], .Lc_extra_%=\n\t"
    ".Lc_tab_%=:\n\t"
    "s_mul_i32 s63, s52, 6144\n\t"
    "v_lshl_add_u32 v4, v0, 4, s63\n\t"
    "s_mov_b32 s64, 131072\n\t"
    "v_add_u32 v8, 0, v4\n\t"
    "v_min_u32 v8, 23024, v8\n\t"
    "buffer_load_dwordx4 v[16:19], v8, s[40:43], s64 offen sc1\n\t"
    "v_add_u32 v9, 1024, v4\n\t"
    "v_min_u32 v9, 23024, v9\n\t"
    "buffer_load_dwordx4 v[20:23], v9, s[40:43], s64 offen sc1\n\t"
    "v_add_u32 v10, 2048, v4\n\t"
    "v_min_u32 v10, 23024, v10\n\t"
    "buffer_load_dwordx4 v[24:27], v10, s[40:43], s64 offen sc1\n\t"
    "v_add_u32 v11, 3072, v4\n\t"
    "v_min_u32 v11, 23024, v11\n\t"
    "buffer_load_dwordx4 v[28:31], v11, s[40:43], s64 offen sc1\n\t"
    "v_add_u32 v12, 4096, v4\n\t"
    "v_min_u32 v12, 23024, v12\n\t"
    "buffer_load_dwordx4 v[32:35], v12, s[40:43], s64 offen sc1\n\t"
    "v_add_u32 v13, 5120, v4\n\t"
    "v_min_u32 v13, 23024, v13\n\t"
    "buffer_load_dwordx4 v[36:39], v13, s[40:43], s64 offen sc1\n\t"
    "s_waitcnt vmcnt(0)\n\t"
    "v_min3_u32 v5, v16, v17, v18\n\t"
    "v_min3_u32 v5, v5, v19, v20\n\t"
    "v_min3_u32 v5, v5, v21, v22\n\t"
    "v_min3_u32 v5, v5, v23, v24\n\t"
    "v_min3_u32 v5, v5, v25, v26\n\t"
    "v_min3_u32 v5, v5, v27, v28\n\t"
    "v_min3_u32 v5, v5, v29, v30\n\t"
    "v_min3_u32 v5, v5, v31, v32\n\t"
    "v_min3_u32 v5, v5, v33, v34\n\t"
    "v_min3_u32 v5, v5, v35, v36\n\t"
    "v_min3_u32 v5, v5, v37, v38\n\t"
    "v_min_u32 v5, v5, v39\n\t"
    "v_cmp_eq_u32 vcc, 0, v5\n\t"
    "s_cbranch_vccz .Lc_tabok_%=\n\t"
    "s_add_u32 s58, s58, 1\n\t"
    "s_cmp_gt_u32 s58, 0x20000\n\t"
    "s_cbranch_scc1 .Lc_tabok_%=\n\t"
    "s_sleep 4\n\t"
    "s_branch .Lc_tab_%=\n\t"
    ".Lc_tabok_%=:\n\t"
    "ds_write_b128 v8, v[16:19] offset:29696\n\t"
    "ds_write_b128 v9, v[20:23] offset:29696\n\t"
    "ds_write_b128 v10, v[24:27] offset:29696\n\t"
    "ds_write_b128 v11, v[28:31] offset:29696\n\t"
    "ds_write_b128 v12, v[32:35] offset:29696\n\t"
    "ds_write_b128 v13, v[36:39] offset:29696\n\t"
    "s_barrier\n\t"
    "s_or_b32 s63, s53, s52\n\t"
    "s_cmp_lg_u32 s63, 0\n\t"
    "s_cbranch_scc1 .Lc_f3_%=\n\t"
    "v_mov_b32 v6, s34\n\t"
    "v_mov_b32 v7, s35\n\t"
    "v_mov_b32 v4, 0\n\t"
    "s_mov_b32 s64, 3137536\n\t"
    "s_mov_b64 exec, 1\n\t"
    "buffer_store_dwordx2 v[6:7], v4, s[40:43], s64 offen sc1\n\t"
    "s_mov_b64 exec, -1\n\t"
    ".Lc_f3_%=:\n\t"
    "s_mov_b32 s54, 0\n\t"
    ".Lc_loop_%=:\n\t"
    "s_mov_b32 s55, s54\n\t"
    "s_mov_b32 s56, 256\n\t"
    "s_cmp_eq_u32 s54, 7\n\t"
    "s_cselect_b32 s56, 256, s56\n\t"
    "s_mov_b32 s59, 0\n\t"
    "s_mov_b32 s38, 14336\n\t"
    "s_call_b64 s[68:69], .Lc_head_%=\n\t"
    "s_mov_b32 s39, s54\n\t"
    "s_call_b64 s[68:69], .Lc_mid_%=\n\t"
    "s_waitcnt lgkmcnt(0)\n\t"
    "s_barrier\n\t"
    "s_call_b64 s[68:69], .Lc_extra_%=\n\t"
    "s_cmp_eq_u32 s54, 7\n\t"
    "s_cbranch_scc1 .Lc_tail_%=\n\t"
    "s_waitcnt vmcnt(0)\n\t"
    "s_waitcnt lgkmcnt(0)\n\t"
    "s_barrier\n\t"
    "s_call_b64 s[68:69], .Lc_xdma_%=\n\t"
    "v_mov_b32 v8, 0\n\t"
    "v_mov_b32 v9, 0\n\t"
    "v_mov_b32 v10, 0\n\t"
    "v_mov_b32 v11, 0\n\t"
    "v_lshlrev_b32 v4, 4, v0\n\t"
    "s_mul_i32 s63, s54, 28672\n\t"
    "s_add_u32 s63, s63, s33\n\t"
    "s_add_u32 s64, s63, 57344\n\t"
    "s_add_u32 s63, s63, 229376\n\t"
    "s_mov_b64 exec, 0xfffffff\n\t"
    "buffer_store_dwordx4 v[8:11], v4, s[40:43], s64 offen sc1\n\t"
    "buffer_store_dwordx4 v[8:11], v4, s[40:43], s63 offen sc1\n\t"
    "s_cmp_lg_u32 s54, 0\n\t"
    "s_cbranch_scc1 .Lc_z1_%=\n\t"
    "s_mov_b64 exec, -1\n\t"
    "v_and_b32 v5, 31, v0\n\t"
    "v_lshlrev_b32 v5, 4, v5\n\t"
    "v_lshrrev_b32 v6, 5, v0\n\t"
    "v_lshl_add_u32 v5, v6, 13, v5\n\t"
    "s_lshl_b32 s64, s52, 14\n\t"
    "s_lshl_b32 s65, s53, 9\n\t"
    "s_add_u32 s64, s64, s65\n\t"
    "s_add_u32 s64, s64, 614400\n\t"
    "buffer_store_dwordx4 v[8:11], v5, s[40:43], s64 offen sc1\n\t"
    ".Lc_z1_%=:\n\t"
    "s_mov_b64 exec, -1\n\t"
    "s_add_u32 s54, s54, 1\n\t"
    "s_branch .Lc_loop_%=\n\t"
    ".Lc_tail_%=:\n\t"
    "s_lshl_b32 s63, s52, 1\n\t"
    "v_lshrrev_b32 v4, 5, v0\n\t"
    "v_add_u32 v4, s63, v4\n\t"
    "v_min_u32 v4, 6, v4\n\t"
    "v_and_b32 v5, 31, v0\n\t"
    "v_lshlrev_b32 v5, 4, v5\n\t"
    "v_lshl_add_u32 v6, v4, 13, v5\n\t"
    "v_lshl_add_u32 v5, v4, 9, v5\n\t"
    "s_lshl_b32 s64, s53, 9\n\t"
    "s_add_u32 s64, s64, 614400\n\t"
    ".Lc_t1_%=:\n\t"
    "buffer_load_dwordx4 v[8:11], v6, s[40:43], s64 offen sc1\n\t"
    "s_waitcnt vmcnt(0)\n\t"
    "v_min3_u32 v4, v8, v9, v10\n\t"
    "v_min_u32 v4, v4, v11\n\t"
    "v_cmp_eq_u32 vcc, 0, v4\n\t"
    "s_cbranch_vccz .Lc_t2_%=\n\t"
    "s_add_u32 s58, s58, 1\n\t"
    "s_cmp_gt_u32 s58, 0x20000\n\t"
    "s_cbranch_scc1 .Lc_t2_%=\n\t"
    "s_sleep 2\n\t"
    "s_branch .Lc_t1_%=\n\t"
    ".Lc_t2_%=:\n\t"
    "ds_write_b128 v5, v[8:11] offset:14848\n\t"
    "s_mov_b32 s59, 1\n\t"
    "s_call_b64 s[68:69], .Lc_head_%=\n\t"
    "s_mov_b32 s55, 8\n\t"
    "s_mov_b32 s59, 0\n\t"
    "s_mov_b32 s54, 15\n\t"
    "s_mov_b32 s56, 256\n\t"
    "s_call_b64 s[68:69], .Lc_head_%=\n\t"
    "s_mov_b32 s59, 1\n\t"
    "s_bitcmp1_b32 s53, 0\n\t"
    "s_cbranch_scc1 .Lc_end_%=\n\t"
    "s_lshr_b32 s39, s53, 1\n\t"
    "s_add_u32 s39, s39, 8\n\t"
    "s_call_b64 s[68:69], .Lc_mid_%=\n\t"
    ".Lc_end_%=:\n\t"
    "s_endpgm\n\t"
    ".Lc_head_%=:\n\t"
    "s_lshr_b32 s57, s55, 1\n\t"
    "s_add_u32 s57, s57, 1\n\t"
    "s_and_b32 s57, s57, 1\n\t"
    "s_and_b32 s64, s55, 1\n\t"
    "s_lshl_b32 s64, s64, 15\n\t"
    "s_add_u32 s64, s64, s36\n\t"
    "s_add_u32 s62, s64, 679936\n\t"
    "s_lshl_b32 s60, s64, 3\n\t"
    "s_add_u32 s60, s60, 778240\n\t"
    "s_lshl_b32 s64, s52, 7\n\t"
    "s_add_u32 s60, s60, s64\n\t"
    "s_add_u32 s85, s60, 8192\n\t"
    "s_add_u32 s86, s60, 16384\n\t"
    "s_add_u32 s87, s60, 24576\n\t"
    "v_lshrrev_b32 v8, 3, v0\n\t"
    "v_and_b32 v9, 7, v0\n\t"
    "v_lshlrev_b32 v9, 4, v9\n\t"
    "v_lshlrev_b32 v4, 7, v8\n\t"
    "v_lshl_add_u32 v5, v8, 10, v9\n\t"
    "v_lshrrev_b32 v8, 4, v0\n\t"
    "s_add_u32 s63, s38, s64\n\t"
    "v_lshl_add_u32 v6, v8, 2, v9\n\t"
    "v_add_u32 v6, s63, v6\n\t"
    "v_and_b32 v9, 15, v0\n\t"
    "s_cmp_eq_u32 s59, 0\n\t"
    "s_cselect_b32 s63, 0, 7\n\t"
    "s_cselect_b32 s64, s38, 14848\n\t"
    "s_cselect_b32 s65, s54, 8\n\t"
    "s_cselect_b32 s94, 0x00010001, 0\n\t"
    "s_cselect_b32 s94, s94, 0x00ff00ff\n\t"
    "s_mov_b32 s95, s94\n\t"
    "v_and_b32 v7, s63, v9\n\t"
    "v_lshlrev_b32 v3, 9, v7\n\t"
    "v_lshl_add_u32 v3, v8, 4, v3\n\t"
    "v_add_u32 v7, s64, v3\n\t"
    "s_lshl_b32 s64, s52, 12\n\t"
    "s_add_u32 s64, s64, 65536\n\t"
    "v_add_u32 v3, s64, v3\n\t"
    "v_lshl_add_u32 v2, v0, 4, s64\n\t"
    "v_and_b32 v9, 31, v0\n\t"
    "v_lshrrev_b32 v8, 5, v0\n\t"
    "v_mul_u32_u24 v1, 28672, v8\n\t"
    "v_lshl_add_u32 v1, v9, 4, v1\n\t"
    "v_cmp_gt_u32 vcc, 28, v9\n\t"
    "v_bfrev_b32 v10, 1\n\t"
    "s_nop 0\n\t"
    "v_cndmask_b32 v1, v10, v1, vcc\n\t"
    "s_lshl_b32 s65, s65, 4\n\t"
    "s_add_u32 s65, s65, s53\n\t"
    "s_mul_i32 s65, s65, 1792\n\t"
    "s_mul_i32 s64, s52, 448\n\t"
    "s_add_u32 s65, s65, s64\n\t"
    "s_add_u32 s65, s65, 155648\n\t"
    "s_cmp_eq_u32 s59, 1\n\t"
    "s_cbranch_scc1 .Lc_h6_%=\n\t"
    "s_cmp_ge_u32 s56, 256\n\t"
    "s_cbranch_scc1 .Lc_h2_%=\n\t"
    ".Lc_h1_%=:\n\t"
    "buffer_load_dwordx2 v[24:25], v4, s[40:43], s62 offen offset:0 sc1\n\t"
    "buffer_load_dwordx2 v[26:27], v4, s[40:43], s62 offen offset:1024 sc1\n\t"
    "buffer_load_dwordx2 v[28:29], v4, s[40:43], s62 offen offset:2048 sc1\n\t"
    "buffer_load_dwordx2 v[30:31], v4, s[40:43], s62 offen offset:3072 sc1\n\t"
    "s_mov_b32 s63, 0\n\t"
    "s_waitcnt vmcnt(0)\n\t"
    "v_bitop3_b32 v8, v24, s57, 1 bitop3:0x28\n\t"
    "v_cmp_ne_u32 vcc, 0, v8\n\t"
    "s_bcnt1_i32_b64 s64, vcc\n\t"
    "s_add_u32 s63, s63, s64\n\t"
    "v_bitop3_b32 v8, v26, s57, 1 bitop3:0x28\n\t"
    "v_cmp_ne_u32 vcc, 0, v8\n\t"
    "s_bcnt1_i32_b64 s64, vcc\n\t"
    "s_add_u32 s63, s63, s64\n\t"
    "v_bitop3_b32 v8, v28, s57, 1 bitop3:0x28\n\t"
    "v_cmp_ne_u32 vcc, 0, v8\n\t"
    "s_bcnt1_i32_b64 s64, vcc\n\t"
    "s_add_u32 s63, s63, s64\n\t"
    "v_bitop3_b32 v8, v30, s57, 1 bitop3:0x28\n\t"
    "v_cmp_ne_u32 vcc, 0, v8\n\t"
    "s_bcnt1_i32_b64 s64, vcc\n\t"
    "s_add_u32 s63, s63, s64\n\t"
    "s_cmp_le_u32 s63, s56\n\t"
    "s_cbranch_scc1 .Lc_h2_%=\n\t"
    "s_add_u32 s58, s58, 1\n\t"
    "s_cmp_gt_u32 s58, 0x20000\n\t"
    "s_cbranch_scc1 .Lc_h2_%=\n\t"
    "s_sleep 2\n\t"
    "s_branch .Lc_h1_%=\n\t"
    ".Lc_h2_%=:\n\t"
    "buffer_load_dwordx2 v[24:25], v4, s[40:43], s62 offen offset:0 sc1\n\t"
    "buffer_load_dwordx2 v[26:27], v4, s[40:43], s62 offen offset:1024 sc1\n\t"
    "buffer_load_dwordx2 v[28:29], v4, s[40:43], s62 offen offset:2048 sc1\n\t"
    "buffer_load_dwordx2 v[30:31], v4, s[40:43], s62 offen offset:3072 sc1\n\t"
    "buffer_load_dwordx4 v[32:35], v5, s[40:43], s60 offen sc1\n\t"
    "buffer_load_dwordx4 v[36:39], v5, s[40:43], s85 offen sc1\n\t"
    "buffer_load_dwordx4 v[40:43], v5, s[40:43], s86 offen sc1\n\t"
    "buffer_load_dwordx4 v[44:47], v5, s[40:43], s87 offen sc1\n\t"
    "s_waitcnt vmcnt(4)\n\t"
    "v_bitop3_b32 v8, v24, v25, s57 bitop3:0x7e\n\t"
    "v_bitop3_b32 v9, v26, v27, s57 bitop3:0x7e\n\t"
    "v_bitop3_b32 v10, v28, v29, s57 bitop3:0x7e\n\t"
    "v_bitop3_b32 v11, v30, v31, s57 bitop3:0x7e\n\t"
    "v_or3_b32 v8, v8, v9, v10\n\t"
    "v_bitop3_b32 v8, v8, v11, 1 bitop3:0xa8\n\t"
    "v_cmp_ne_u32 s[92:93], 0, v8\n\t"
    "v_max3_f32 v20, v24, v26, v28\n\t"
    "v_max_f32 v20, v20, v30\n\t"
    "s_nop 1\n\t"
    "v_max_f32_dpp v20, v20, v20 row_ror:8 row_mask:0xf bank_mask:0xf\n\t"
    "v_mov_b32 v21, v20\n\t"
    "s_nop 1\n\t"
    "v_permlane16_swap_b32 v20, v21\n\t"
    "v_max_f32 v20, v20, v21\n\t"
    "v_mov_b32 v21, v20\n\t"
    "s_nop 1\n\t"
    "v_permlane32_swap_b32 v20, v21\n\t"
    "v_max_f32 v20, v20, v21\n\t"
    "v_sub_f32 v24, v24, v20\n\t"
    "v_sub_f32 v26, v26, v20\n\t"
    "v_sub_f32 v28, v28, v20\n\t"
    "v_sub_f32 v30, v30, v20\n\t"
    "v_exp_f32 v24, v24\n\t"
    "v_exp_f32 v26, v26\n\t"
    "v_exp_f32 v28, v28\n\t"
    "v_exp_f32 v30, v30\n\t"
    "v_mul_f32 v22, v24, v25\n\t"
    "v_fmac_f32 v22, v26, v27\n\t"
    "v_fmac_f32 v22, v28, v29\n\t"
    "v_fmac_f32 v22, v30, v31\n\t"
    "s_waitcnt vmcnt(3)\n\t"
    "v_bitop3_b32 v8, v32, v33, s57 bitop3:0x7e\n\t"
    "v_bitop3_b32 v9, v34, v35, s57 bitop3:0x7e\n\t"
    "v_bitop3_b32 v8, v8, v9, 1 bitop3:0xa8\n\t"
    "v_cmp_ne_u32 s[76:77], 0, v8\n\t"
    "s_or_b64 s[92:93], s[92:93], s[76:77]\n\t"
    "v_lshlrev_b32 v56, 16, v32\n\t"
    "v_and_b32 v57, 0xffff0000, v32\n\t"
    "v_lshlrev_b32 v58, 16, v33\n\t"
    "v_and_b32 v59, 0xffff0000, v33\n\t"
    "v_lshlrev_b32 v60, 16, v34\n\t"
    "v_and_b32 v61, 0xffff0000, v34\n\t"
    "v_lshlrev_b32 v62, 16, v35\n\t"
    "v_and_b32 v63, 0xffff0000, v35\n\t"
    "v_pk_mul_f32 v[48:49], v[56:57], v[24:25] op_sel_hi:[1,0]\n\t"
    "v_pk_mul_f32 v[50:51], v[58:59], v[24:25] op_sel_hi:[1,0]\n\t"
    "v_pk_mul_f32 v[52:53], v[60:61], v[24:25] op_sel_hi:[1,0]\n\t"
    "v_pk_mul_f32 v[54:55], v[62:63], v[24:25] op_sel_hi:[1,0]\n\t"
    "s_waitcnt vmcnt(2)\n\t"
    "v_bitop3_b32 v8, v36, v37, s57 bitop3:0x7e\n\t"
    "v_bitop3_b32 v9, v38, v39, s57 bitop3:0x7e\n\t"
    "v_bitop3_b32 v8, v8, v9, 1 bitop3:0xa8\n\t"
    "v_cmp_ne_u32 s[78:79], 0, v8\n\t"
    "s_or_b64 s[92:93], s[92:93], s[78:79]\n\t"
    "v_lshlrev_b32 v56, 16, v36\n\t"
    "v_and_b32 v57, 0xffff0000, v36\n\t"
    "v_lshlrev_b32 v58, 16, v37\n\t"
    "v_and_b32 v59, 0xffff0000, v37\n\t"
    "v_lshlrev_b32 v60, 16, v38\n\t"
    "v_and_b32 v61, 0xffff0000, v38\n\t"
    "v_lshlrev_b32 v62, 16, v39\n\t"
    "v_and_b32 v63, 0xffff0000, v39\n\t"
    "v_pk_fma_f32 v[48:49], v[56:57], v[26:27], v[48:49] op_sel_hi:[1,0,1]\n\t"
    "v_pk_fma_f32 v[50:51], v[58:59], v[26:27], v[50:51] op_sel_hi:[1,0,1]\n\t"
    "v_pk_fma_f32 v[52:53], v[60:61], v[26:27], v[52:53] op_sel_hi:[1,0,1]\n\t"
    "v_pk_fma_f32 v[54:55], v[62:63], v[26:27], v[54:55] op_sel_hi:[1,0,1]\n\t"
    "s_waitcnt vmcnt(1)\n\t"
    "v_bitop3_b32 v8, v40, v41, s57 bitop3:0x7e\n\t"
    "v_bitop3_b32 v9, v42, v43, s57 bitop3:0x7e\n\t"
    "v_bitop3_b32 v8, v8, v9, 1 bitop3:0xa8\n\t"
    "v_cmp_ne_u32 s[80:81], 0, v8\n\t"
    "s_or_b64 s[92:93], s[92:93], s[80:81]\n\t"
    "v_lshlrev_b32 v56, 16, v40\n\t"
    "v_and_b32 v57, 0xffff0000, v40\n\t"
    "v_lshlrev_b32 v58, 16, v41\n\t"
    "v_and_b32 v59, 0xffff0000, v41\n\t"
    "v_lshlrev_b32 v60, 16, v42\n\t"
    "v_and_b32 v61, 0xffff0000, v42\n\t"
    "v_lshlrev_b32 v62, 16, v43\n\t"
    "v_and_b32 v63, 0xffff0000, v43\n\t"
    "v_pk_fma_f32 v[48:49], v[56:57], v[28:29], v[48:49] op_sel_hi:[1,0,1]\n\t"
    "v_pk_fma_f32 v[50:51], v[58:59], v[28:29], v[50:51] op_sel_hi:[1,0,1]\n\t"
    "v_pk_fma_f32 v[52:53], v[60:61], v[28:29], v[52:53] op_sel_hi:[1,0,1]\n\t"
    "v_pk_fma_f32 v[54:55], v[62:63], v[28:29], v[54:55] op_sel_hi:[1,0,1]\n\t"
    "s_waitcnt vmcnt(0)\n\t"
    "v_bitop3_b32 v8, v44, v45, s57 bitop3:0x7e\n\t"
    "v_bitop3_b32 v9, v46, v47, s57 bitop3:0x7e\n\t"
    "v_bitop3_b32 v8, v8, v9, 1 bitop3:0xa8\n\t"
    "v_cmp_ne_u32 s[82:83], 0, v8\n\t"
    "s_or_b64 s[92:93], s[92:93], s[82:83]\n\t"
    "v_lshlrev_b32 v56, 16, v44\n\t"
    "v_and_b32 v57, 0xffff0000, v44\n\t"
    "v_lshlrev_b32 v58, 16, v45\n\t"
    "v_and_b32 v59, 0xffff0000, v45\n\t"
    "v_lshlrev_b32 v60, 16, v46\n\t"
    "v_and_b32 v61, 0xffff0000, v46\n\t"
    "v_lshlrev_b32 v62, 16, v47\n\t"
    "v_and_b32 v63, 0xffff0000, v47\n\t"
    "v_pk_fma_f32 v[48:49], v[56:57], v[30:31], v[48:49] op_sel_hi:[1,0,1]\n\t"
    "v_pk_fma_f32 v[50:51], v[58:59], v[30:31], v[50:51] op_sel_hi:[1,0,1]\n\t"
    "v_pk_fma_f32 v[52:53], v[60:61], v[30:31], v[52:53] op_sel_hi:[1,0,1]\n\t"
    "v_pk_fma_f32 v[54:55], v[62:63], v[30:31], v[54:55] op_sel_hi:[1,0,1]\n\t"
    "s_cmp_eq_u64 s[92:93], 0\n\t"
    "s_cbranch_scc1 .Lc_h4_%=\n\t"
    ".Lc_h3_%=:\n\t"
    "buffer_load_dwordx2 v[24:25], v4, s[40:43], s62 offen offset:0 sc1\n\t"
    "buffer_load_dwordx2 v[26:27], v4, s[40:43], s62 offen offset:1024 sc1\n\t"
    "buffer_load_dwordx2 v[28:29], v4, s[40:43], s62 offen offset:2048 sc1\n\t"
    "buffer_load_dwordx2 v[30:31], v4, s[40:43], s62 offen offset:3072 sc1\n\t"
    "s_mov_b64 exec, s[76:77]\n\t"
    "s_cbranch_execz .Lc_h3s0_%=\n\t"
    "buffer_load_dwordx4 v[32:35], v5, s[40:43], s60 offen sc1\n\t"
    ".Lc_h3s0_%=:\n\t"
    "s_mov_b64 exec, s[78:79]\n\t"
    "s_cbranch_execz .Lc_h3s1_%=\n\t"
    "buffer_load_dwordx4 v[36:39], v5, s[40:43], s85 offen sc1\n\t"
    ".Lc_h3s1_%=:\n\t"
    "s_mov_b64 exec, s[80:81]\n\t"
    "s_cbranch_execz .Lc_h3s2_%=\n\t"
    "buffer_load_dwordx4 v[40:43], v5, s[40:43], s86 offen sc1\n\t"
    ".Lc_h3s2_%=:\n\t"
    "s_mov_b64 exec, s[82:83]\n\t"
    "s_cbranch_execz .Lc_h3s3_%=\n\t"
    "buffer_load_dwordx4 v[44:47], v5, s[40:43], s87 offen sc1\n\t"
    ".Lc_h3s3_%=:\n\t"
    "s_mov_b64 exec, -1\n\t"
    "s_waitcnt vmcnt(0)\n\t"
    "v_bitop3_b32 v8, v24, v25, s57 bitop3:0x7e\n\t"
    "v_bitop3_b32 v9, v26, v27, s57 bitop3:0x7e\n\t"
    "v_bitop3_b32 v10, v28, v29, s57 bitop3:0x7e\n\t"
    "v_bitop3_b32 v11, v30, v31, s57 bitop3:0x7e\n\t"
    "v_or3_b32 v8, v8, v9, v10\n\t"
    "v_bitop3_b32 v8, v8, v11, 1 bitop3:0xa8\n\t"
    "v_cmp_ne_u32 s[92:93], 0, v8\n\t"
    "v_bitop3_b32 v8, v32, v33, s57 bitop3:0x7e\n\t"
    "v_bitop3_b32 v9, v34, v35, s57 bitop3:0x7e\n\t"
    "v_bitop3_b32 v8, v8, v9, 1 bitop3:0xa8\n\t"
    "v_cmp_ne_u32 s[76:77], 0, v8\n\t"
    "s_or_b64 s[92:93], s[92:93], s[76:77]\n\t"
    "v_bitop3_b32 v8, v36, v37, s57 bitop3:0x7e\n\t"
    "v_bitop3_b32 v9, v38, v39, s57 bitop3:0x7e\n\t"
    "v_bitop3_b32 v8, v8, v9, 1 bitop3:0xa8\n\t"
    "v_cmp_ne_u32 s[78:79], 0, v8\n\t"
    "s_or_b64 s[92:93], s[92:93], s[78:79]\n\t"
    "v_bitop3_b32 v8, v40, v41, s57 bitop3:0x7e\n\t"
    "v_bitop3_b32 v9, v42, v43, s57 bitop3:0x7e\n\t"
    "v_bitop3_b32 v8, v8, v9, 1 bitop3:0xa8\n\t"
    "v_cmp_ne_u32 s[80:81], 0, v8\n\t"
    "s_or_b64 s[92:93], s[92:93], s[80:81]\n\t"
    "v_bitop3_b32 v8, v44, v45, s57 bitop3:0x7e\n\t"
    "v_bitop3_b32 v9, v46, v47, s57 bitop3:0x7e\n\t"
    "v_bitop3_b32 v8, v8, v9, 1 bitop3:0xa8\n\t"
    "v_cmp_ne_u32 s[82:83], 0, v8\n\t"
    "s_or_b64 s[92:93], s[92:93], s[82:83]\n\t"
    "s_cmp_eq_u64 s[92:93], 0\n\t"
    "s_cbranch_scc1 .Lc_h3ok_%=\n\t"
    "s_add_u32 s58, s58, 1\n\t"
    "s_cmp_gt_u32 s58, 0x20000\n\t"
    "s_cbranch_scc1 .Lc_h3ok_%=\n\t"
    "s_branch .Lc_h3_%=\n\t"
    ".Lc_h3ok_%=:\n\t"
    "v_max3_f32 v20, v24, v26, v28\n\t"
    "v_max_f32 v20, v20, v30\n\t"
    "s_nop 1\n\t"
    "v_max_f32_dpp v20, v20, v20 row_ror:8 row_mask:0xf bank_mask:0xf\n\t"
    "v_mov_b32 v21, v20\n\t"
    "s_nop 1\n\t"
    "v_permlane16_swap_b32 v20, v21\n\t"
    "v_max_f32 v20, v20, v21\n\t"
    "v_mov_b32 v21, v20\n\t"
    "s_nop 1\n\t"
    "v_permlane32_swap_b32 v20, v21\n\t"
    "v_max_f32 v20, v20, v21\n\t"
    "v_sub_f32 v24, v24, v20\n\t"
    "v_sub_f32 v26, v26, v20\n\t"
    "v_sub_f32 v28, v28, v20\n\t"
    "v_sub_f32 v30, v30, v20\n\t"
    "v_exp_f32 v24, v24\n\t"
    "v_exp_f32 v26, v26\n\t"
    "v_exp_f32 v28, v28\n\t"
    "v_exp_f32 v30, v30\n\t"
    "v_mul_f32 v22, v24, v25\n\t"
    "v_fmac_f32 v22, v26, v27\n\t"
    "v_fmac_f32 v22, v28, v29\n\t"
    "v_fmac_f32 v22, v30, v31\n\t"
    "v_lshlrev_b32 v56, 16, v32\n\t"
    "v_and_b32 v57, 0xffff0000, v32\n\t"
    "v_lshlrev_b32 v58, 16, v33\n\t"
    "v_and_b32 v59, 0xffff0000, v33\n\t"
    "v_lshlrev_b32 v60, 16, v34\n\t"
    "v_and_b32 v61, 0xffff0000, v34\n\t"
    "v_lshlrev_b32 v62, 16, v35\n\t"
    "v_and_b32 v63, 0xffff0000, v35\n\t"
    "v_pk_mul_f32 v[48:49], v[56:57], v[24:25] op_sel_hi:[1,0]\n\t"
    "v_pk_mul_f32 v[50:51], v[58:59], v[24:25] op_sel_hi:[1,0]\n\t"
    "v_pk_mul_f32 v[52:53], v[60:61], v[24:25] op_sel_hi:[1,0]\n\t"
    "v_pk_mul_f32 v[54:55], v[62:63], v[24:25] op_sel_hi:[1,0]\n\t"
    "v_lshlrev_b32 v56, 16, v36\n\t"
    "v_and_b32 v57, 0xffff0000, v36\n\t"
    "v_lshlrev_b32 v58, 16, v37\n\t"
    "v_and_b32 v59, 0xffff0000, v37\n\t"
    "v_lshlrev_b32 v60, 16, v38\n\t"
    "v_and_b32 v61, 0xffff0000, v38\n\t"
    "v_lshlrev_b32 v62, 16, v39\n\t"
    "v_and_b32 v63, 0xffff0000, v39\n\t"
    "v_pk_fma_f32 v[48:49], v[56:57], v[26:27], v[48:49] op_sel_hi:[1,0,1]\n\t"
    "v_pk_fma_f32 v[50:51], v[58:59], v[26:27], v[50:51] op_sel_hi:[1,0,1]\n\t"
    "v_pk_fma_f32 v[52:53], v[60:61], v[26:27], v[52:53] op_sel_hi:[1,0,1]\n\t"
    "v_pk_fma_f32 v[54:55], v[62:63], v[26:27], v[54:55] op_sel_hi:[1,0,1]\n\t"
    "v_lshlrev_b32 v56, 16, v40\n\t"
    "v_and_b32 v57, 0xffff0000, v40\n\t"
    "v_lshlrev_b32 v58, 16, v41\n\t"
    "v_and_b32 v59, 0xffff0000, v41\n\t"
    "v_lshlrev_b32 v60, 16, v42\n\t"
    "v_and_b32 v61, 0xffff0000, v42\n\t"
    "v_lshlrev_b32 v62, 16, v43\n\t"
    "v_and_b32 v63, 0xffff0000, v43\n\t"
    "v_pk_fma_f32 v[48:49], v[56:57], v[28:29], v[48:49] op_sel_hi:[1,0,1]\n\t"
    "v_pk_fma_f32 v[50:51], v[58:59], v[28:29], v[50:51] op_sel_hi:[1,0,1]\n\t"
    "v_pk_fma_f32 v[52:53], v[60:61], v[28:29], v[52:53] op_sel_hi:[1,0,1]\n\t"
    "v_pk_fma_f32 v[54:55], v[62:63], v[28:29], v[54:55] op_sel_hi:[1,0,1]\n\t"
    "v_lshlrev_b32 v56, 16, v44\n\t"
    "v_and_b32 v57, 0xffff0000, v44\n\t"
    "v_lshlrev_b32 v58, 16, v45\n\t"
    "v_and_b32 v59, 0xffff0000, v45\n\t"
    "v_lshlrev_b32 v60, 16, v46\n\t"
    "v_and_b32 v61, 0xffff0000, v46\n\t"
    "v_lshlrev_b32 v62, 16, v47\n\t"
    "v_and_b32 v63, 0xffff0000, v47\n\t"
    "v_pk_fma_f32 v[48:49], v[56:57], v[30:31], v[48:49] op_sel_hi:[1,0,1]\n\t"
    "v_pk_fma_f32 v[50:51], v[58:59], v[30:31], v[50:51] op_sel_hi:[1,0,1]\n\t"
    "v_pk_fma_f32 v[52:53], v[60:61], v[30:31], v[52:53] op_sel_hi:[1,0,1]\n\t"
    "v_pk_fma_f32 v[54:55], v[62:63], v[30:31], v[54:55] op_sel_hi:[1,0,1]\n\t"
    ".Lc_h4_%=:\n\t"
    "v_mov_b32 v23, v22\n\t"
    "s_nop 0\n\t"
    "v_permlane32_swap_b32 v48, v52\n\t"
    "v_permlane32_swap_b32 v49, v53\n\t"
    "v_permlane32_swap_b32 v50, v54\n\t"
    "v_permlane32_swap_b32 v51, v55\n\t"
    "v_permlane32_swap_b32 v22, v23\n\t"
    "v_pk_add_f32 v[48:49], v[48:49], v[52:53]\n\t"
    "v_pk_add_f32 v[50:51], v[50:51], v[54:55]\n\t"
    "v_add_f32 v22, v22, v23\n\t"
    "v_mov_b32 v23, v22\n\t"
    "v_permlane16_swap_b32 v48, v50\n\t"
    "v_permlane16_swap_b32 v49, v51\n\t"
    "v_permlane16_swap_b32 v22, v23\n\t"
    "v_add_f32 v22, v22, v23\n\t"
    "v_pk_add_f32 v[48:49], v[48:49], v[50:51]\n\t"
    "s_nop 0\n\t"
    "v_add_f32_dpp v22, v22, v22 row_ror:8 row_mask:0xf bank_mask:0xf\n\t"
    "v_add_f32_dpp v48, v48, v48 row_ror:8 row_mask:0xf bank_mask:0xf\n\t"
    "v_rcp_f32 v22, v22\n\t"
    "v_add_f32_dpp v49, v49, v49 row_ror:8 row_mask:0xf bank_mask:0xf\n\t"
    "v_pk_mul_f32 v[48:49], v[48:49], v[22:23] op_sel_hi:[1,0]\n\t"
    "v_cvt_pk_bf16_f32 v48, v48, v49\n\t"
    "ds_write_b32 v6, v48\n\t"
    ".Lc_h6_%=:\n\t"
    "s_waitcnt lgkmcnt(0)\n\t"
    "s_barrier\n\t"
    "ds_read_b128 v[32:35], v7 offset:0\n\t"
    "ds_read_b128 v[36:39], v7 offset:64\n\t"
    "ds_read_b128 v[40:43], v7 offset:128\n\t"
    "ds_read_b128 v[44:47], v7 offset:192\n\t"
    "ds_read_b128 v[48:51], v7 offset:256\n\t"
    "ds_read_b128 v[52:55], v7 offset:320\n\t"
    "ds_read_b128 v[56:59], v7 offset:384\n\t"
    "ds_read_b128 v[60:63], v7 offset:448\n\t"
    "s_waitcnt lgkmcnt(7)\n\t"
    "v_mfma_f32_16x16x32_bf16 v[8:11], a[224:227], v[32:35], 0\n\t"
    "s_waitcnt lgkmcnt(6)\n\t"
    "v_mfma_f32_16x16x32_bf16 v[8:11], a[252:255], v[36:39], v[8:11]\n\t"
    "s_waitcnt lgkmcnt(5)\n\t"
    "v_mfma_f32_16x16x32_bf16 v[8:11], v[88:91], v[40:43], v[8:11]\n\t"
    "s_waitcnt lgkmcnt(4)\n\t"
    "v_mfma_f32_16x16x32_bf16 v[8:11], v[116:119], v[44:47], v[8:11]\n\t"
    "s_waitcnt lgkmcnt(3)\n\t"
    "v_mfma_f32_16x16x32_bf16 v[8:11], v[144:147], v[48:51], v[8:11]\n\t"
    "s_waitcnt lgkmcnt(2)\n\t"
    "v_mfma_f32_16x16x32_bf16 v[8:11], v[172:175], v[52:55], v[8:11]\n\t"
    "s_waitcnt lgkmcnt(1)\n\t"
    "v_mfma_f32_16x16x32_bf16 v[8:11], v[200:203], v[56:59], v[8:11]\n\t"
    "s_waitcnt lgkmcnt(0)\n\t"
    "v_mfma_f32_16x16x32_bf16 v[8:11], v[228:231], v[60:63], v[8:11]\n\t"
    "v_mfma_f32_16x16x32_bf16 v[12:15], a[228:231], v[32:35], 0\n\t"
    "v_mfma_f32_16x16x32_bf16 v[12:15], v[64:67], v[36:39], v[12:15]\n\t"
    "v_mfma_f32_16x16x32_bf16 v[12:15], v[92:95], v[40:43], v[12:15]\n\t"
    "v_mfma_f32_16x16x32_bf16 v[12:15], v[120:123], v[44:47], v[12:15]\n\t"
    "v_mfma_f32_16x16x32_bf16 v[12:15], v[148:151], v[48:51], v[12:15]\n\t"
    "v_mfma_f32_16x16x32_bf16 v[12:15], v[176:179], v[52:55], v[12:15]\n\t"
    "v_mfma_f32_16x16x32_bf16 v[12:15], v[204:207], v[56:59], v[12:15]\n\t"
    "v_mfma_f32_16x16x32_bf16 v[12:15], v[232:235], v[60:63], v[12:15]\n\t"
    "v_mfma_f32_16x16x32_bf16 v[16:19], a[232:235], v[32:35], 0\n\t"
    "v_mfma_f32_16x16x32_bf16 v[16:19], v[68:71], v[36:39], v[16:19]\n\t"
    "s_mov_b64 exec, s[94:95]\n\t"
    "ds_write_b128 v3, v[8:11] offset:0\n\t"
    "s_mov_b64 exec, -1\n\t"
    "v_mfma_f32_16x16x32_bf16 v[16:19], v[96:99], v[40:43], v[16:19]\n\t"
    "v_mfma_f32_16x16x32_bf16 v[16:19], v[124:127], v[44:47], v[16:19]\n\t"
    "v_mfma_f32_16x16x32_bf16 v[16:19], v[152:155], v[48:51], v[16:19]\n\t"
    "v_mfma_f32_16x16x32_bf16 v[16:19], v[180:183], v[52:55], v[16:19]\n\t"
    "v_mfma_f32_16x16x32_bf16 v[16:19], v[208:211], v[56:59], v[16:19]\n\t"
    "v_mfma_f32_16x16x32_bf16 v[16:19], v[236:239], v[60:63], v[16:19]\n\t"
    "v_mfma_f32_16x16x32_bf16 v[8:11], a[236:239], v[32:35], 0\n\t"
    "v_mfma_f32_16x16x32_bf16 v[8:11], v[72:75], v[36:39], v[8:11]\n\t"
    "s_mov_b64 exec, s[94:95]\n\t"
    "ds_write_b128 v3, v[12:15] offset:64\n\t"
    "s_mov_b64 exec, -1\n\t"
    "v_mfma_f32_16x16x32_bf16 v[8:11], v[100:103], v[40:43], v[8:11]\n\t"
    "v_mfma_f32_16x16x32_bf16 v[8:11], v[128:131], v[44:47], v[8:11]\n\t"
    "v_mfma_f32_16x16x32_bf16 v[8:11], v[156:159], v[48:51], v[8:11]\n\t"
    "v_mfma_f32_16x16x32_bf16 v[8:11], v[184:187], v[52:55], v[8:11]\n\t"
    "v_mfma_f32_16x16x32_bf16 v[8:11], v[212:215], v[56:59], v[8:11]\n\t"
    "v_mfma_f32_16x16x32_bf16 v[8:11], v[240:243], v[60:63], v[8:11]\n\t"
    "v_mfma_f32_16x16x32_bf16 v[12:15], a[240:243], v[32:35], 0\n\t"
    "v_mfma_f32_16x16x32_bf16 v[12:15], v[76:79], v[36:39], v[12:15]\n\t"
    "s_mov_b64 exec, s[94:95]\n\t"
    "ds_write_b128 v3, v[16:19] offset:128\n\t"
    "s_mov_b64 exec, -1\n\t"
    "v_mfma_f32_16x16x32_bf16 v[12:15], v[104:107], v[40:43], v[12:15]\n\t"
    "v_mfma_f32_16x16x32_bf16 v[12:15], v[132:135], v[44:47], v[12:15]\n\t"
    "v_mfma_f32_16x16x32_bf16 v[12:15], v[160:163], v[48:51], v[12:15]\n\t"
    "v_mfma_f32_16x16x32_bf16 v[12:15], v[188:191], v[52:55], v[12:15]\n\t"
    "v_mfma_f32_16x16x32_bf16 v[12:15], v[216:219], v[56:59], v[12:15]\n\t"
    "v_mfma_f32_16x16x32_bf16 v[12:15], v[244:247], v[60:63], v[12:15]\n\t"
    "v_mfma_f32_16x16x32_bf16 v[16:19], a[244:247], v[32:35], 0\n\t"
    "v_mfma_f32_16x16x32_bf16 v[16:19], v[80:83], v[36:39], v[16:19]\n\t"
    "s_mov_b64 exec, s[94:95]\n\t"
    "ds_write_b128 v3, v[8:11] offset:192\n\t"
    "s_mov_b64 exec, -1\n\t"
    "v_mfma_f32_16x16x32_bf16 v[16:19], v[108:111], v[40:43], v[16:19]\n\t"
    "v_mfma_f32_16x16x32_bf16 v[16:19], v[136:139], v[44:47], v[16:19]\n\t"
    "v_mfma_f32_16x16x32_bf16 v[16:19], v[164:167], v[48:51], v[16:19]\n\t"
    "v_mfma_f32_16x16x32_bf16 v[16:19], v[192:195], v[52:55], v[16:19]\n\t"
    "v_mfma_f32_16x16x32_bf16 v[16:19], v[220:223], v[56:59], v[16:19]\n\t"
    "v_mfma_f32_16x16x32_bf16 v[16:19], v[248:251], v[60:63], v[16:19]\n\t"
    "v_mfma_f32_16x16x32_bf16 v[8:11], a[248:251], v[32:35], 0\n\t"
    "v_mfma_f32_16x16x32_bf16 v[8:11], v[84:87], v[36:39], v[8:11]\n\t"
    "s_mov_b64 exec, s[94:95]\n\t"
    "ds_write_b128 v3, v[12:15] offset:256\n\t"
    "s_mov_b64 exec, -1\n\t"
    "v_mfma_f32_16x16x32_bf16 v[8:11], v[112:115], v[40:43], v[8:11]\n\t"
    "v_mfma_f32_16x16x32_bf16 v[8:11], v[140:143], v[44:47], v[8:11]\n\t"
    "v_mfma_f32_16x16x32_bf16 v[8:11], v[168:171], v[48:51], v[8:11]\n\t"
    "v_mfma_f32_16x16x32_bf16 v[8:11], v[196:199], v[52:55], v[8:11]\n\t"
    "v_mfma_f32_16x16x32_bf16 v[8:11], v[224:227], v[56:59], v[8:11]\n\t"
    "v_mfma_f32_16x16x32_bf16 v[8:11], v[252:255], v[60:63], v[8:11]\n\t"
    "s_mov_b64 exec, s[94:95]\n\t"
    "ds_write_b128 v3, v[16:19] offset:320\n\t"
    "s_mov_b64 exec, -1\n\t"
    "s_nop 3\n\t"
    "s_mov_b64 exec, s[94:95]\n\t"
    "ds_write_b128 v3, v[8:11] offset:384\n\t"
    "s_mov_b64 exec, -1\n\t"
    "s_cmp_eq_u32 s59, 0\n\t"
    "s_cbranch_scc0 .Lc_h5_%=\n\t"
    "ds_read_b128 v[20:23], v2\n\t"
    "s_mov_b32 exec_hi, 0\n\t"
    "s_waitcnt lgkmcnt(0)\n\t"
    "v_max_u32 v20, 1, v20\n\t"
    "v_max_u32 v21, 1, v21\n\t"
    "v_max_u32 v22, 1, v22\n\t"
    "v_max_u32 v23, 1, v23\n\t"
    "buffer_store_dwordx4 v[20:23], v1, s[40:43], s65 offen sc1\n\t"
    "s_mov_b32 exec_hi, -1\n\t"
    "s_setpc_b64 s[68:69]\n\t"
    ".Lc_h5_%=:\n\t"
    "ds_read_b128 v[44:47], v2 offset:3072\n\t"
    "ds_read_b128 v[40:43], v2 offset:2048\n\t"
    "ds_read_b128 v[36:39], v2 offset:1024\n\t"
    "ds_read_b128 v[32:35], v2 offset:0\n\t"
    "s_add_u32 s64, s65, 172032\n\t"
    "s_waitcnt lgkmcnt(3)\n\t"
    "v_max_u32 v44, 1, v44\n\t"
    "v_max_u32 v45, 1, v45\n\t"
    "v_max_u32 v46, 1, v46\n\t"
    "v_max_u32 v47, 1, v47\n\t"
    "s_mov_b32 exec_hi, 0\n\t"
    "buffer_store_dwordx4 v[44:47], v1, s[40:43], s64 offen sc1\n\t"
    "s_mov_b32 exec_hi, -1\n\t"
    "s_add_u32 s64, s65, 114688\n\t"
    "s_waitcnt lgkmcnt(2)\n\t"
    "v_max_u32 v40, 1, v40\n\t"
    "v_max_u32 v41, 1, v41\n\t"
    "v_max_u32 v42, 1, v42\n\t"
    "v_max_u32 v43, 1, v43\n\t"
    "buffer_store_dwordx4 v[40:43], v1, s[40:43], s64 offen sc1\n\t"
    "s_add_u32 s64, s65, 57344\n\t"
    "s_waitcnt lgkmcnt(1)\n\t"
    "v_max_u32 v36, 1, v36\n\t"
    "v_max_u32 v37, 1, v37\n\t"
    "v_max_u32 v38, 1, v38\n\t"
    "v_max_u32 v39, 1, v39\n\t"
    "buffer_store_dwordx4 v[36:39], v1, s[40:43], s64 offen sc1\n\t"
    "s_add_u32 s64, s65, 0\n\t"
    "s_waitcnt lgkmcnt(0)\n\t"
    "v_max_u32 v32, 1, v32\n\t"
    "v_max_u32 v33, 1, v33\n\t"
    "v_max_u32 v34, 1, v34\n\t"
    "v_max_u32 v35, 1, v35\n\t"
    "buffer_store_dwordx4 v[32:35], v1, s[40:43], s64 offen sc1\n\t"
    "s_setpc_b64 s[68:69]\n\t"
    ".Lc_mid_%=:\n\t"
    "s_lshl_b32 s63, s52, 6\n\t"
    "v_add_u32 v4, s63, v0\n\t"
    "v_lshlrev_b32 v2, 4, v4\n\t"
    "v_cmp_le_u32 vcc, 112, v4\n\t"
    "v_mov_b32 v5, 12544\n\t"
    "s_nop 0\n\t"
    "v_cndmask_b32 v1, 0, v5, vcc\n\t"
    "v_add_u32 v1, v1, v2\n\t"
    "v_cmp_le_u32 vcc, 224, v4\n\t"
    "v_mov_b32 v5, 10240\n\t"
    "s_nop 0\n\t"
    "v_cndmask_b32 v1, v1, 0, vcc\n\t"
    "v_cndmask_b32 v2, v2, v5, vcc\n\t"
    "s_mul_i32 s60, s39, 28672\n\t"
    "s_add_u32 s60, s60, 155648\n\t"
    "s_add_u32 s61, s60, 3584\n\t"
    "s_add_u32 s62, s60, 7168\n\t"
    "s_add_u32 s65, s60, 10752\n\t"
    "v_lshlrev_b32 v3, 5, v0\n\t"
    ".Lc_m1_%=:\n\t"
    "buffer_load_dwordx4 v[32:35], v1, s[40:43], s60 offen offset:0 sc1\n\t"
    "buffer_load_dwordx4 v[36:39], v1, s[40:43], s60 offen offset:1792 sc1\n\t"
    "buffer_load_dwordx4 v[40:43], v1, s[40:43], s61 offen offset:0 sc1\n\t"
    "buffer_load_dwordx4 v[44:47], v1, s[40:43], s61 offen offset:1792 sc1\n\t"
    "buffer_load_dwordx4 v[48:51], v1, s[40:43], s62 offen offset:0 sc1\n\t"
    "buffer_load_dwordx4 v[52:55], v1, s[40:43], s62 offen offset:1792 sc1\n\t"
    "buffer_load_dwordx4 v[56:59], v1, s[40:43], s65 offen offset:0 sc1\n\t"
    "buffer_load_dwordx4 v[60:63], v1, s[40:43], s65 offen offset:1792 sc1\n\t"
    "s_cmp_eq_u32 s59, 0\n\t"
    "s_cbranch_scc0 .Lc_m2_%=\n\t"
    "s_mul_i32 s63, s54, 896\n\t"
    "v_lshl_add_u32 v4, v0, 4, s63\n\t"
    "ds_read_b128 v[16:19], v4 offset:7168\n\t"
    "s_waitcnt lgkmcnt(0)\n\t"
    "v_lshlrev_b32 v8, 16, v16\n\t"
    "v_and_b32 v9, 0xffff0000, v16\n\t"
    "v_lshlrev_b32 v10, 16, v17\n\t"
    "v_and_b32 v11, 0xffff0000, v17\n\t"
    "v_lshlrev_b32 v12, 16, v18\n\t"
    "v_and_b32 v13, 0xffff0000, v18\n\t"
    "v_lshlrev_b32 v14, 16, v19\n\t"
    "v_and_b32 v15, 0xffff0000, v19\n\t"
    "s_branch .Lc_m3_%=\n\t"
    ".Lc_m2_%=:\n\t"
    "ds_read_b128 v[8:11], v3 offset:22784\n\t"
    "ds_read_b128 v[12:15], v3 offset:22800\n\t"
    ".Lc_m3_%=:\n\t"
    "s_waitcnt vmcnt(7)\n\t"
    "v_min3_u32 v4, v32, v33, v34\n\t"
    "v_min_u32 v4, v4, v35\n\t"
    "s_waitcnt vmcnt(6)\n\t"
    "v_min3_u32 v4, v4, v36, v37\n\t"
    "v_min3_u32 v4, v4, v38, v39\n\t"
    "v_pk_add_f32 v[16:17], v[32:33], v[36:37]\n\t"
    "v_pk_add_f32 v[18:19], v[34:35], v[38:39]\n\t"
    "s_waitcnt vmcnt(5)\n\t"
    "v_min3_u32 v4, v4, v40, v41\n\t"
    "v_min3_u32 v4, v4, v42, v43\n\t"
    "v_pk_add_f32 v[16:17], v[16:17], v[40:41]\n\t"
    "v_pk_add_f32 v[18:19], v[18:19], v[42:43]\n\t"
    "s_waitcnt vmcnt(4)\n\t"
    "v_min3_u32 v4, v4, v44, v45\n\t"
    "v_min3_u32 v4, v4, v46, v47\n\t"
    "v_pk_add_f32 v[16:17], v[16:17], v[44:45]\n\t"
    "v_pk_add_f32 v[18:19], v[18:19], v[46:47]\n\t"
    "s_waitcnt vmcnt(3)\n\t"
    "v_min3_u32 v4, v4, v48, v49\n\t"
    "v_min3_u32 v4, v4, v50, v51\n\t"
    "v_pk_add_f32 v[16:17], v[16:17], v[48:49]\n\t"
    "v_pk_add_f32 v[18:19], v[18:19], v[50:51]\n\t"
    "s_waitcnt vmcnt(2)\n\t"
    "v_min3_u32 v4, v4, v52, v53\n\t"
    "v_min3_u32 v4, v4, v54, v55\n\t"
    "v_pk_add_f32 v[16:17], v[16:17], v[52:53]\n\t"
    "v_pk_add_f32 v[18:19], v[18:19], v[54:55]\n\t"
    "s_waitcnt vmcnt(1)\n\t"
    "v_min3_u32 v4, v4, v56, v57\n\t"
    "v_min3_u32 v4, v4, v58, v59\n\t"
    "v_pk_add_f32 v[16:17], v[16:17], v[56:57]\n\t"
    "v_pk_add_f32 v[18:19], v[18:19], v[58:59]\n\t"
    "s_waitcnt vmcnt(0)\n\t"
    "v_min3_u32 v4, v4, v60, v61\n\t"
    "v_min3_u32 v4, v4, v62, v63\n\t"
    "v_pk_add_f32 v[16:17], v[16:17], v[60:61]\n\t"
    "v_pk_add_f32 v[18:19], v[18:19], v[62:63]\n\t"
    "v_cmp_eq_u32 vcc, 0, v4\n\t"
    "s_cbranch_vccz .Lc_m4_%=\n\t"
    "s_add_u32 s58, s58, 1\n\t"
    "s_cmp_gt_u32 s58, 0x20000\n\t"
    "s_cbranch_scc1 .Lc_m4_%=\n\t"
    "s_branch .Lc_m1_%=\n\t"
    ".Lc_m4_%=:\n\t"
    "ds_write_b128 v2, v[16:19] offset:18944\n\t"
    "s_waitcnt lgkmcnt(0)\n\t"
    "s_barrier\n\t"
    "ds_read_b128 v[16:19], v3 offset:18944\n\t"
    "ds_read_b128 v[20:23], v3 offset:18960\n\t"
    "ds_read_b128 v[24:27], v3 offset:20736\n\t"
    "ds_read_b128 v[28:31], v3 offset:20752\n\t"
    "s_mov_b32 s63, 0x6d006d00\n\t"
    "s_mov_b32 s64, 0x16001600\n\t"
    "s_waitcnt lgkmcnt(2)\n\t"
    "v_pk_add_f32 v[8:9], v[8:9], v[16:17]\n\t"
    "v_pk_add_f32 v[10:11], v[10:11], v[18:19]\n\t"
    "v_pk_add_f32 v[12:13], v[12:13], v[20:21]\n\t"
    "v_pk_add_f32 v[14:15], v[14:15], v[22:23]\n\t"
    "s_waitcnt lgkmcnt(0)\n\t"
    "v_pk_add_f32 v[8:9], v[8:9], v[24:25]\n\t"
    "v_pk_add_f32 v[10:11], v[10:11], v[26:27]\n\t"
    "v_pk_add_f32 v[12:13], v[12:13], v[28:29]\n\t"
    "v_pk_add_f32 v[14:15], v[14:15], v[30:31]\n\t"
    "s_nop 0\n\t"
    "v_readfirstlane_b32 s74, v8\n\t"
    "s_nop 1\n\t"
    "v_pk_add_f32 v[16:17], v[8:9], s[74:75] op_sel_hi:[1,0] neg_lo:[0,1] neg_hi:[0,1]\n\t"
    "v_pk_add_f32 v[18:19], v[10:11], s[74:75] op_sel_hi:[1,0] neg_lo:[0,1] neg_hi:[0,1]\n\t"
    "v_pk_add_f32 v[20:21], v[12:13], s[74:75] op_sel_hi:[1,0] neg_lo:[0,1] neg_hi:[0,1]\n\t"
    "v_pk_add_f32 v[22:23], v[14:15], s[74:75] op_sel_hi:[1,0] neg_lo:[0,1] neg_hi:[0,1]\n\t"
    "v_pk_add_f32 v[24:25], v[16:17], v[18:19]\n\t"
    "v_pk_mul_f32 v[26:27], v[16:17], v[16:17]\n\t"
    "v_pk_add_f32 v[28:29], v[20:21], v[22:23]\n\t"
    "v_pk_fma_f32 v[26:27], v[18:19], v[18:19], v[26:27]\n\t"
    "v_pk_add_f32 v[24:25], v[24:25], v[28:29]\n\t"
    "v_pk_fma_f32 v[26:27], v[20:21], v[20:21], v[26:27]\n\t"
    "v_add_f32 v24, v24, v25\n\t"
    "v_pk_fma_f32 v[26:27], v[22:23], v[22:23], v[26:27]\n\t"
    "v_cndmask_b32 v24, 0, v24, s[72:73]\n\t"
    "v_add_f32 v26, v26, v27\n\t"
    "v_cndmask_b32 v26, 0, v26, s[72:73]\n\t"
    "s_nop 1\n\t"
    "v_permlane16_swap_b32 v24, v26\n\t"
    "v_add_f32 v24, v24, v26\n\t"
    "v_mov_b32 v25, v24\n\t"
    "s_nop 1\n\t"
    "v_permlane32_swap_b32 v24, v25\n\t"
    "v_add_f32 v24, v24, v25\n\t"
    "s_nop 1\n\t"
    "v_add_f32_dpp v24, v24, v24 quad_perm:[1,0,3,2] row_mask:0xf bank_mask:0xf\n\t"
    "s_nop 1\n\t"
    "v_add_f32_dpp v24, v24, v24 quad_perm:[2,3,0,1] row_mask:0xf bank_mask:0xf\n\t"
    "s_nop 1\n\t"
    "v_add_f32_dpp v24, v24, v24 row_half_mirror row_mask:0xf bank_mask:0xf\n\t"
    "s_nop 1\n\t"
    "v_add_f32_dpp v24, v24, v24 row_mirror row_mask:0xf bank_mask:0xf\n\t"
    "v_mul_f32 v24, 0x3b124925, v24\n\t"
    "s_nop 0\n\t"
    "v_readlane_b32 s74, v24, 0\n\t"
    "v_readlane_b32 s75, v24, 16\n\t"
    "v_mov_b32 v27, 0x3727c5ac\n\t"
    "v_mov_b32 v25, s74\n\t"
    "v_mov_b32 v26, s75\n\t"
    "v_fma_f32 v26, -v25, v25, v26\n\t"
    "v_max_f32 v26, 0, v26\n\t"
    "v_add_f32 v24, v26, v27\n\t"
    "v_rsq_f32 v24, v24\n\t"
    "s_nop 0\n\t"
    "v_mul_f32 v26, v26, v24\n\t"
    "v_fma_f32 v26, v26, v24, v27\n\t"
    "v_rsq_f32 v26, v26\n\t"
    "s_nop 0\n\t"
    "v_mul_f32 v24, v24, v26\n\t"
    "v_mul_f32 v25, -v25, v24\n\t"
    "v_pk_fma_f32 v[16:17], v[16:17], v[24:25], v[24:25] op_sel:[0,0,1] op_sel_hi:[1,0,1]\n\t"
    "v_pk_fma_f32 v[18:19], v[18:19], v[24:25], v[24:25] op_sel:[0,0,1] op_sel_hi:[1,0,1]\n\t"
    "v_pk_fma_f32 v[20:21], v[20:21], v[24:25], v[24:25] op_sel:[0,0,1] op_sel_hi:[1,0,1]\n\t"
    "v_pk_fma_f32 v[22:23], v[22:23], v[24:25], v[24:25] op_sel:[0,0,1] op_sel_hi:[1,0,1]\n\t"
    "v_cvt_pk_bf16_f32 v24, v16, v17\n\t"
    "v_cvt_pk_bf16_f32 v25, v18, v19\n\t"
    "v_cvt_pk_bf16_f32 v26, v20, v21\n\t"
    "v_cvt_pk_bf16_f32 v27, v22, v23\n\t"
    "v_pk_lshlrev_b16 v28, 1, v24 op_sel_hi:[0,1]\n\t"
    "v_pk_lshrrev_b16 v40, 15, v24 op_sel_hi:[0,1]\n\t"
    "v_pk_lshlrev_b16 v29, 1, v25 op_sel_hi:[0,1]\n\t"
    "v_pk_lshrrev_b16 v41, 15, v25 op_sel_hi:[0,1]\n\t"
    "v_pk_lshlrev_b16 v30, 1, v26 op_sel_hi:[0,1]\n\t"
    "v_pk_lshrrev_b16 v42, 15, v26 op_sel_hi:[0,1]\n\t"
    "v_pk_lshlrev_b16 v31, 1, v27 op_sel_hi:[0,1]\n\t"
    "v_pk_lshrrev_b16 v43, 15, v27 op_sel_hi:[0,1]\n\t"
    "v_pk_sub_u16 v28, v28, s63\n\t"
    "v_pk_sub_u16 v29, v29, s63\n\t"
    "v_pk_sub_u16 v30, v30, s63\n\t"
    "v_pk_sub_u16 v31, v31, s63\n\t"
    "v_pk_min_u16 v28, v28, s64\n\t"
    "v_pk_min_u16 v29, v29, s64\n\t"
    "v_pk_min_u16 v30, v30, s64\n\t"
    "v_pk_min_u16 v31, v31, s64\n\t"
    "v_or_b32 v28, v28, v40\n\t"
    "v_or_b32 v29, v29, v41\n\t"
    "v_or_b32 v30, v30, v42\n\t"
    "v_or_b32 v31, v31, v43\n\t"
    "v_lshlrev_b32_sdwa v32, 2, v28 dst_sel:DWORD dst_unused:UNUSED_PAD src0_sel:DWORD src1_sel:WORD_0\n\t"
    "ds_read_b32 v16, v32 offset:29696\n\t"
    "v_lshlrev_b32_sdwa v33, 2, v28 dst_sel:DWORD dst_unused:UNUSED_PAD src0_sel:DWORD src1_sel:WORD_1\n\t"
    "ds_read_b32 v17, v33 offset:29696\n\t"
    "v_lshlrev_b32_sdwa v34, 2, v29 dst_sel:DWORD dst_unused:UNUSED_PAD src0_sel:DWORD src1_sel:WORD_0\n\t"
    "ds_read_b32 v18, v34 offset:29696\n\t"
    "v_lshlrev_b32_sdwa v35, 2, v29 dst_sel:DWORD dst_unused:UNUSED_PAD src0_sel:DWORD src1_sel:WORD_1\n\t"
    "ds_read_b32 v19, v35 offset:29696\n\t"
    "v_lshlrev_b32_sdwa v36, 2, v30 dst_sel:DWORD dst_unused:UNUSED_PAD src0_sel:DWORD src1_sel:WORD_0\n\t"
    "ds_read_b32 v20, v36 offset:29696\n\t"
    "v_lshlrev_b32_sdwa v37, 2, v30 dst_sel:DWORD dst_unused:UNUSED_PAD src0_sel:DWORD src1_sel:WORD_1\n\t"
    "ds_read_b32 v21, v37 offset:29696\n\t"
    "v_lshlrev_b32_sdwa v38, 2, v31 dst_sel:DWORD dst_unused:UNUSED_PAD src0_sel:DWORD src1_sel:WORD_0\n\t"
    "ds_read_b32 v22, v38 offset:29696\n\t"
    "v_lshlrev_b32_sdwa v39, 2, v31 dst_sel:DWORD dst_unused:UNUSED_PAD src0_sel:DWORD src1_sel:WORD_1\n\t"
    "ds_read_b32 v23, v39 offset:29696\n\t"
    "v_max3_u32 v4, v32, v33, v34\n\t"
    "v_max3_u32 v4, v4, v35, v36\n\t"
    "v_max3_u32 v4, v4, v37, v38\n\t"
    "v_max_u32 v4, v4, v39\n\t"
    "v_cmp_le_u32 vcc, 22528, v4\n\t"
    "s_and_b64 vcc, vcc, s[72:73]\n\t"
    "s_waitcnt lgkmcnt(0)\n\t"
    "s_cbranch_vccnz .Lc_miss_%=\n\t"
    ".Lc_m5_%=:\n\t"
    "v_pk_add_f32 v[8:9], v[8:9], v[16:17]\n\t"
    "v_pk_add_f32 v[10:11], v[10:11], v[18:19]\n\t"
    "v_pk_add_f32 v[12:13], v[12:13], v[20:21]\n\t"
    "v_pk_add_f32 v[14:15], v[14:15], v[22:23]\n\t"
    "s_cmp_eq_u32 s59, 0\n\t"
    "s_cbranch_scc1 .Lc_m6_%=\n\t"
    "v_cvt_pk_bf16_f32 v16, v8, v9\n\t"
    "v_cvt_pk_bf16_f32 v17, v10, v11\n\t"
    "v_cvt_pk_bf16_f32 v18, v12, v13\n\t"
    "v_cvt_pk_bf16_f32 v19, v14, v15\n\t"
    "s_cmp_lg_u32 s52, 0\n\t"
    "s_cbranch_scc1 .Lc_m7_%=\n\t"
    "s_lshr_b32 s63, s53, 1\n\t"
    "s_mul_i32 s63, s63, 896\n\t"
    "v_lshl_add_u32 v4, v0, 4, s63\n\t"
    "s_mov_b64 exec, s[72:73]\n\t"
    "global_store_dwordx4 v4, v[16:19], %10\n\t"
    "s_mov_b64 exec, -1\n\t"
    ".Lc_m7_%=:\n\t"
    "s_barrier\n\t"
    "s_setpc_b64 s[68:69]\n\t"
    ".Lc_m6_%=:\n\t"
    "s_nop 0\n\t"
    "v_readfirstlane_b32 s74, v8\n\t"
    "s_nop 1\n\t"
    "v_pk_add_f32 v[16:17], v[8:9], s[74:75] op_sel_hi:[1,0] neg_lo:[0,1] neg_hi:[0,1]\n\t"
    "v_pk_add_f32 v[18:19], v[10:11], s[74:75] op_sel_hi:[1,0] neg_lo:[0,1] neg_hi:[0,1]\n\t"
    "v_pk_add_f32 v[20:21], v[12:13], s[74:75] op_sel_hi:[1,0] neg_lo:[0,1] neg_hi:[0,1]\n\t"
    "v_pk_add_f32 v[22:23], v[14:15], s[74:75] op_sel_hi:[1,0] neg_lo:[0,1] neg_hi:[0,1]\n\t"
    "v_pk_add_f32 v[24:25], v[16:17], v[18:19]\n\t"
    "v_pk_mul_f32 v[26:27], v[16:17], v[16:17]\n\t"
    "v_pk_add_f32 v[28:29], v[20:21], v[22:23]\n\t"
    "v_pk_fma_f32 v[26:27], v[18:19], v[18:19], v[26:27]\n\t"
    "v_pk_add_f32 v[24:25], v[24:25], v[28:29]\n\t"
    "v_pk_fma_f32 v[26:27], v[20:21], v[20:21], v[26:27]\n\t"
    "v_add_f32 v24, v24, v25\n\t"
    "v_pk_fma_f32 v[26:27], v[22:23], v[22:23], v[26:27]\n\t"
    "v_cndmask_b32 v24, 0, v24, s[72:73]\n\t"
    "v_add_f32 v26, v26, v27\n\t"
    "v_cndmask_b32 v26, 0, v26, s[72:73]\n\t"
    "s_nop 1\n\t"
    "v_permlane16_swap_b32 v24, v26\n\t"
    "v_add_f32 v24, v24, v26\n\t"
    "v_mov_b32 v25, v24\n\t"
    "s_nop 1\n\t"
    "v_permlane32_swap_b32 v24, v25\n\t"
    "v_add_f32 v24, v24, v25\n\t"
    "s_nop 1\n\t"
    "v_add_f32_dpp v24, v24, v24 quad_perm:[1,0,3,2] row_mask:0xf bank_mask:0xf\n\t"
    "s_nop 1\n\t"
    "v_add_f32_dpp v24, v24, v24 quad_perm:[2,3,0,1] row_mask:0xf bank_mask:0xf\n\t"
    "s_nop 1\n\t"
    "v_add_f32_dpp v24, v24, v24 row_half_mirror row_mask:0xf bank_mask:0xf\n\t"
    "s_nop 1\n\t"
    "v_add_f32_dpp v24, v24, v24 row_mirror row_mask:0xf bank_mask:0xf\n\t"
    "v_mul_f32 v24, 0x3b124925, v24\n\t"
    "s_nop 0\n\t"
    "v_readlane_b32 s74, v24, 0\n\t"
    "v_readlane_b32 s75, v24, 16\n\t"
    "v_mov_b32 v27, 0x3727c5ac\n\t"
    "v_mov_b32 v25, s74\n\t"
    "v_mov_b32 v26, s75\n\t"
    "v_fma_f32 v26, -v25, v25, v26\n\t"
    "v_max_f32 v26, 0, v26\n\t"
    "v_add_f32 v24, v26, v27\n\t"
    "v_rsq_f32 v24, v24\n\t"
    "s_nop 0\n\t"
    "v_mul_f32 v25, -v25, v24\n\t"
    "v_pk_fma_f32 v[16:17], v[16:17], v[24:25], v[24:25] op_sel:[0,0,1] op_sel_hi:[1,0,1]\n\t"
    "v_pk_fma_f32 v[18:19], v[18:19], v[24:25], v[24:25] op_sel:[0,0,1] op_sel_hi:[1,0,1]\n\t"
    "v_pk_fma_f32 v[20:21], v[20:21], v[24:25], v[24:25] op_sel:[0,0,1] op_sel_hi:[1,0,1]\n\t"
    "v_pk_fma_f32 v[22:23], v[22:23], v[24:25], v[24:25] op_sel:[0,0,1] op_sel_hi:[1,0,1]\n\t"
    "v_cvt_pk_bf16_f32 v24, v16, v17\n\t"
    "v_cvt_pk_bf16_f32 v25, v18, v19\n\t"
    "v_cvt_pk_bf16_f32 v26, v20, v21\n\t"
    "v_cvt_pk_bf16_f32 v27, v22, v23\n\t"
    "s_mul_i32 s63, s52, 896\n\t"
    "v_lshl_add_u32 v4, v0, 4, s63\n\t"
    "s_mov_b64 exec, s[72:73]\n\t"
    "ds_write_b128 v4, v[24:27] offset:0\n\t"
    "s_lshl_b32 s64, s54, 1\n\t"
    "s_cmp_lg_u32 s64, s53\n\t"
    "s_cbranch_scc1 .Lc_m8_%=\n\t"
    "ds_write_b128 v3, v[8:11] offset:22784\n\t"
    "ds_write_b128 v3, v[12:15] offset:22800\n\t"
    ".Lc_m8_%=:\n\t"
    "s_mov_b64 exec, -1\n\t"
    "v_and_b32 v1, 48, v0\n\t"
    "v_lshlrev_b32 v3, 1, v1\n\t"
    "v_add_u32 v1, s63, v1\n\t"
    "s_lshl_b32 s64, s52, 7\n\t"
    "s_add_u32 s37, s64, 28672\n\t"
    "v_add_u32 v3, s37, v3\n\t"
    "s_lshl_b32 s97, s54, 4\n\t"
    "s_add_u32 s97, s97, s53\n\t"
    "s_lshl_b32 s97, s97, 9\n\t"
    "s_add_u32 s97, s97, s64\n\t"
    "s_add_u32 s97, s97, 0x10000\n\t"
    "s_mov_b64 s[92:93], 0xff\n\t"
    ".Lc_q_%=:\n\t"
    "ds_read_b128 v[20:23], v1 offset:0\n\t"
    "ds_read_b128 v[24:27], v1 offset:64\n\t"
    "ds_read_b128 v[28:31], v1 offset:128\n\t"
    "ds_read_b128 v[32:35], v1 offset:192\n\t"
    "ds_read_b128 v[36:39], v1 offset:256\n\t"
    "ds_read_b128 v[40:43], v1 offset:320\n\t"
    "ds_read_b128 v[44:47], v1 offset:384\n\t"
    "ds_read_b128 v[48:51], v1 offset:448\n\t"
    "ds_read_b128 v[52:55], v1 offset:512\n\t"
    "ds_read_b128 v[56:59], v1 offset:576\n\t"
    "ds_read_b128 v[60:63], v1 offset:640\n\t"
    "s_waitcnt lgkmcnt(10)\n\t"
    "v_mfma_f32_16x16x32_bf16 v[4:7], a[0:3], v[20:23], 0\n\t"
    "v_mfma_f32_16x16x32_bf16 v[8:11], a[4:7], v[20:23], 0\n\t"
    "v_mfma_f32_16x16x32_bf16 v[12:15], a[8:11], v[20:23], 0\n\t"
    "v_mfma_f32_16x16x32_bf16 v[16:19], a[12:15], v[20:23], 0\n\t"
    "ds_read_b128 v[20:23], v1 offset:704\n\t"
    "s_waitcnt lgkmcnt(10)\n\t"
    "v_mfma_f32_16x16x32_bf16 v[4:7], a[16:19], v[24:27], v[4:7]\n\t"
    "v_mfma_f32_16x16x32_bf16 v[8:11], a[20:23], v[24:27], v[8:11]\n\t"
    "v_mfma_f32_16x16x32_bf16 v[12:15], a[24:27], v[24:27], v[12:15]\n\t"
    "v_mfma_f32_16x16x32_bf16 v[16:19], a[28:31], v[24:27], v[16:19]\n\t"
    "ds_read_b128 v[24:27], v1 offset:768\n\t"
    "s_waitcnt lgkmcnt(10)\n\t"
    "v_mfma_f32_16x16x32_bf16 v[4:7], a[32:35], v[28:31], v[4:7]\n\t"
    "v_mfma_f32_16x16x32_bf16 v[8:11], a[36:39], v[28:31], v[8:11]\n\t"
    "v_mfma_f32_16x16x32_bf16 v[12:15], a[40:43], v[28:31], v[12:15]\n\t"
    "v_mfma_f32_16x16x32_bf16 v[16:19], a[44:47], v[28:31], v[16:19]\n\t"
    "ds_read_b128 v[28:31], v1 offset:832\n\t"
    "s_waitcnt lgkmcnt(10)\n\t"
    "v_mfma_f32_16x16x32_bf16 v[4:7], a[48:51], v[32:35], v[4:7]\n\t"
    "v_mfma_f32_16x16x32_bf16 v[8:11], a[52:55], v[32:35], v[8:11]\n\t"
    "v_mfma_f32_16x16x32_bf16 v[12:15], a[56:59], v[32:35], v[12:15]\n\t"
    "v_mfma_f32_16x16x32_bf16 v[16:19], a[60:63], v[32:35], v[16:19]\n\t"
    "s_waitcnt lgkmcnt(9)\n\t"
    "v_mfma_f32_16x16x32_bf16 v[4:7], a[64:67], v[36:39], v[4:7]\n\t"
    "v_mfma_f32_16x16x32_bf16 v[8:11], a[68:71], v[36:39], v[8:11]\n\t"
    "v_mfma_f32_16x16x32_bf16 v[12:15], a[72:75], v[36:39], v[12:15]\n\t"
    "v_mfma_f32_16x16x32_bf16 v[16:19], a[76:79], v[36:39], v[16:19]\n\t"
    "s_waitcnt lgkmcnt(8)\n\t"
    "v_mfma_f32_16x16x32_bf16 v[4:7], a[80:83], v[40:43], v[4:7]\n\t"
    "v_mfma_f32_16x16x32_bf16 v[8:11], a[84:87], v[40:43], v[8:11]\n\t"
    "v_mfma_f32_16x16x32_bf16 v[12:15], a[88:91], v[40:43], v[12:15]\n\t"
    "v_mfma_f32_16x16x32_bf16 v[16:19], a[92:95], v[40:43], v[16:19]\n\t"
    "s_waitcnt lgkmcnt(7)\n\t"
    "v_mfma_f32_16x16x32_bf16 v[4:7], a[96:99], v[44:47], v[4:7]\n\t"
    "v_mfma_f32_16x16x32_bf16 v[8:11], a[100:103], v[44:47], v[8:11]\n\t"
    "v_mfma_f32_16x16x32_bf16 v[12:15], a[104:107], v[44:47], v[12:15]\n\t"
    "v_mfma_f32_16x16x32_bf16 v[16:19], a[108:111], v[44:47], v[16:19]\n\t"
    "s_waitcnt lgkmcnt(6)\n\t"
    "v_mfma_f32_16x16x32_bf16 v[4:7], a[112:115], v[48:51], v[4:7]\n\t"
    "v_mfma_f32_16x16x32_bf16 v[8:11], a[116:119], v[48:51], v[8:11]\n\t"
    "v_mfma_f32_16x16x32_bf16 v[12:15], a[120:123], v[48:51], v[12:15]\n\t"
    "v_mfma_f32_16x16x32_bf16 v[16:19], a[124:127], v[48:51], v[16:19]\n\t"
    "s_waitcnt lgkmcnt(5)\n\t"
    "v_mfma_f32_16x16x32_bf16 v[4:7], a[128:131], v[52:55], v[4:7]\n\t"
    "v_mfma_f32_16x16x32_bf16 v[8:11], a[132:135], v[52:55], v[8:11]\n\t"
    "v_mfma_f32_16x16x32_bf16 v[12:15], a[136:139], v[52:55], v[12:15]\n\t"
    "v_mfma_f32_16x16x32_bf16 v[16:19], a[140:143], v[52:55], v[16:19]\n\t"
    "s_waitcnt lgkmcnt(4)\n\t"
    "v_mfma_f32_16x16x32_bf16 v[4:7], a[144:147], v[56:59], v[4:7]\n\t"
    "v_mfma_f32_16x16x32_bf16 v[8:11], a[148:151], v[56:59], v[8:11]\n\t"
    "v_mfma_f32_16x16x32_bf16 v[12:15], a[152:155], v[56:59], v[12:15]\n\t"
    "v_mfma_f32_16x16x32_bf16 v[16:19], a[156:159], v[56:59], v[16:19]\n\t"
    "s_waitcnt lgkmcnt(3)\n\t"
    "v_mfma_f32_16x16x32_bf16 v[4:7], a[160:163], v[60:63], v[4:7]\n\t"
    "v_mfma_f32_16x16x32_bf16 v[8:11], a[164:167], v[60:63], v[8:11]\n\t"
    "v_mfma_f32_16x16x32_bf16 v[12:15], a[168:171], v[60:63], v[12:15]\n\t"
    "v_mfma_f32_16x16x32_bf16 v[16:19], a[172:175], v[60:63], v[16:19]\n\t"
    "s_waitcnt lgkmcnt(2)\n\t"
    "v_mfma_f32_16x16x32_bf16 v[4:7], a[176:179], v[20:23], v[4:7]\n\t"
    "v_mfma_f32_16x16x32_bf16 v[8:11], a[180:183], v[20:23], v[8:11]\n\t"
    "v_mfma_f32_16x16x32_bf16 v[12:15], a[184:187], v[20:23], v[12:15]\n\t"
    "v_mfma_f32_16x16x32_bf16 v[16:19], a[188:191], v[20:23], v[16:19]\n\t"
    "s_waitcnt lgkmcnt(1)\n\t"
    "v_mfma_f32_16x16x32_bf16 v[4:7], a[192:195], v[24:27], v[4:7]\n\t"
    "v_mfma_f32_16x16x32_bf16 v[8:11], a[196:199], v[24:27], v[8:11]\n\t"
    "v_mfma_f32_16x16x32_bf16 v[12:15], a[200:203], v[24:27], v[12:15]\n\t"
    "v_mfma_f32_16x16x32_bf16 v[16:19], a[204:207], v[24:27], v[16:19]\n\t"
    "s_waitcnt lgkmcnt(0)\n\t"
    "v_mfma_f32_16x16x32_bf16 v[4:7], a[208:211], v[28:31], v[4:7]\n\t"
    "v_mfma_f32_16x16x32_bf16 v[8:11], a[212:215], v[28:31], v[8:11]\n\t"
    "v_mfma_f32_16x16x32_bf16 v[12:15], a[216:219], v[28:31], v[12:15]\n\t"
    "v_mfma_f32_16x16x32_bf16 v[16:19], a[220:223], v[28:31], v[16:19]\n\t"
    "v_lshrrev_b32 v1, 3, v0\n\t"
    "v_and_b32 v2, 7, v0\n\t"
    "v_lshlrev_b32 v2, 4, v2\n\t"
    "v_lshl_add_u32 v32, v1, 9, v2\n\t"
    "v_lshl_add_u32 v2, v1, 13, v2\n\t"
    "v_add_u32 v32, s37, v32\n\t"
    "v_cvt_pk_bf16_f32 v20, v4, v5\n\t"
    "v_cvt_pk_bf16_f32 v21, v6, v7\n\t"
    "v_cvt_pk_bf16_f32 v22, v8, v9\n\t"
    "v_cvt_pk_bf16_f32 v23, v10, v11\n\t"
    "v_cvt_pk_bf16_f32 v24, v12, v13\n\t"
    "v_cvt_pk_bf16_f32 v25, v14, v15\n\t"
    "v_cvt_pk_bf16_f32 v26, v16, v17\n\t"
    "v_cvt_pk_bf16_f32 v27, v18, v19\n\t"
    "s_mov_b64 exec, s[94:95]\n\t"
    "ds_write_b128 v3, v[20:23]\n\t"
    "ds_write_b128 v3, v[24:27] offset:16\n\t"
    "s_mov_b64 exec, s[92:93]\n\t"
    "ds_read_b128 v[28:31], v32\n\t"
    "s_waitcnt lgkmcnt(0)\n\t"
    "v_max_u32 v28, 1, v28\n\t"
    "v_max_u32 v29, 1, v29\n\t"
    "v_max_u32 v30, 1, v30\n\t"
    "v_max_u32 v31, 1, v31\n\t"
    "buffer_store_dwordx4 v[28:31], v2, s[40:43], s97 offen sc1\n\t"
    "s_mov_b64 exec, -1\n\t"
    "s_setpc_b64 s[68:69]\n\t"
    ".Lc_miss_%=:\n\t"
    "v_cmp_le_u32 vcc, 22528, v32\n\t"
    "s_and_b64 s[92:93], vcc, s[72:73]\n\t"
    ".Lc_ms0_%=:\n\t"
    "s_cmp_eq_u64 s[92:93], 0\n\t"
    "s_cbranch_scc1 .Lc_mt0_%=\n\t"
    "s_ff1_i32_b64 s63, s[92:93]\n\t"
    "s_bitset0_b64 s[92:93], s63\n\t"
    "v_readlane_b32 s64, v24, s63\n\t"
    "s_lshl_b32 s64, s64, 16\n\t"
    "s_call_b64 s[70:71], .Lc_eval_%=\n\t"
    "s_mov_b32 m0, s63\n\t"
    "s_nop 3\n\t"
    "v_writelane_b32 v16, s64, m0\n\t"
    "s_branch .Lc_ms0_%=\n\t"
    ".Lc_mt0_%=:\n\t"
    "v_cmp_le_u32 vcc, 22528, v33\n\t"
    "s_and_b64 s[92:93], vcc, s[72:73]\n\t"
    ".Lc_ms1_%=:\n\t"
    "s_cmp_eq_u64 s[92:93], 0\n\t"
    "s_cbranch_scc1 .Lc_mt1_%=\n\t"
    "s_ff1_i32_b64 s63, s[92:93]\n\t"
    "s_bitset0_b64 s[92:93], s63\n\t"
    "v_readlane_b32 s64, v24, s63\n\t"
    "s_and_b32 s64, s64, 0xffff0000\n\t"
    "s_call_b64 s[70:71], .Lc_eval_%=\n\t"
    "s_mov_b32 m0, s63\n\t"
    "s_nop 3\n\t"
    "v_writelane_b32 v17, s64, m0\n\t"
    "s_branch .Lc_ms1_%=\n\t"
    ".Lc_mt1_%=:\n\t"
    "v_cmp_le_u32 vcc, 22528, v34\n\t"
    "s_and_b64 s[92:93], vcc, s[72:73]\n\t"
    ".Lc_ms2_%=:\n\t"
    "s_cmp_eq_u64 s[92:93], 0\n\t"
    "s_cbranch_scc1 .Lc_mt2_%=\n\t"
    "s_ff1_i32_b64 s63, s[92:93]\n\t"
    "s_bitset0_b64 s[92:93], s63\n\t"
    "v_readlane_b32 s64, v25, s63\n\t"
    "s_lshl_b32 s64, s64, 16\n\t"
    "s_call_b64 s[70:71], .Lc_eval_%=\n\t"
    "s_mov_b32 m0, s63\n\t"
    "s_nop 3\n\t"
    "v_writelane_b32 v18, s64, m0\n\t"
    "s_branch .Lc_ms2_%=\n\t"
    ".Lc_mt2_%=:\n\t"
    "v_cmp_le_u32 vcc, 22528, v35\n\t"
    "s_and_b64 s[92:93], vcc, s[72:73]\n\t"
    ".Lc_ms3_%=:\n\t"
    "s_cmp_eq_u64 s[92:93], 0\n\t"
    "s_cbranch_scc1 .Lc_mt3_%=\n\t"
    "s_ff1_i32_b64 s63, s[92:93]\n\t"
    "s_bitset0_b64 s[92:93], s63\n\t"
    "v_readlane_b32 s64, v25, s63\n\t"
    "s_and_b32 s64, s64, 0xffff0000\n\t"
    "s_call_b64 s[70:71], .Lc_eval_%=\n\t"
    "s_mov_b32 m0, s63\n\t"
    "s_nop 3\n\t"
    "v_writelane_b32 v19, s64, m0\n\t"
    "s_branch .Lc_ms3_%=\n\t"
    ".Lc_mt3_%=:\n\t"
    "v_cmp_le_u32 vcc, 22528, v36\n\t"
    "s_and_b64 s[92:93], vcc, s[72:73]\n\t"
    ".Lc_ms4_%=:\n\t"
    "s_cmp_eq_u64 s[92:93], 0\n\t"
    "s_cbranch_scc1 .Lc_mt4_%=\n\t"
    "s_ff1_i32_b64 s63, s[92:93]\n\t"
    "s_bitset0_b64 s[92:93], s63\n\t"
    "v_readlane_b32 s64, v26, s63\n\t"
    "s_lshl_b32 s64, s64, 16\n\t"
    "s_call_b64 s[70:71], .Lc_eval_%=\n\t"
    "s_mov_b32 m0, s63\n\t"
    "s_nop 3\n\t"
    "v_writelane_b32 v20, s64, m0\n\t"
    "s_branch .Lc_ms4_%=\n\t"
    ".Lc_mt4_%=:\n\t"
    "v_cmp_le_u32 vcc, 22528, v37\n\t"
    "s_and_b64 s[92:93], vcc, s[72:73]\n\t"
    ".Lc_ms5_%=:\n\t"
    "s_cmp_eq_u64 s[92:93], 0\n\t"
    "s_cbranch_scc1 .Lc_mt5_%=\n\t"
    "s_ff1_i32_b64 s63, s[92:93]\n\t"
    "s_bitset0_b64 s[92:93], s63\n\t"
    "v_readlane_b32 s64, v26, s63\n\t"
    "s_and_b32 s64, s64, 0xffff0000\n\t"
    "s_call_b64 s[70:71], .Lc_eval_%=\n\t"
    "s_mov_b32 m0, s63\n\t"
    "s_nop 3\n\t"
    "v_writelane_b32 v21, s64, m0\n\t"
    "s_branch .Lc_ms5_%=\n\t"
    ".Lc_mt5_%=:\n\t"
    "v_cmp_le_u32 vcc, 22528, v38\n\t"
    "s_and_b64 s[92:93], vcc, s[72:73]\n\t"
    ".Lc_ms6_%=:\n\t"
    "s_cmp_eq_u64 s[92:93], 0\n\t"
    "s_cbranch_scc1 .Lc_mt6_%=\n\t"
    "s_ff1_i32_b64 s63, s[92:93]\n\t"
    "s_bitset0_b64 s[92:93], s63\n\t"
    "v_readlane_b32 s64, v27, s63\n\t"
    "s_lshl_b32 s64, s64, 16\n\t"
    "s_call_b64 s[70:71], .Lc_eval_%=\n\t"
    "s_mov_b32 m0, s63\n\t"
    "s_nop 3\n\t"
    "v_writelane_b32 v22, s64, m0\n\t"
    "s_branch .Lc_ms6_%=\n\t"
    ".Lc_mt6_%=:\n\t"
    "v_cmp_le_u32 vcc, 22528, v39\n\t"
    "s_and_b64 s[92:93], vcc, s[72:73]\n\t"
    ".Lc_ms7_%=:\n\t"
    "s_cmp_eq_u64 s[92:93], 0\n\t"
    "s_cbranch_scc1 .Lc_mt7_%=\n\t"
    "s_ff1_i32_b64 s63, s[92:93]\n\t"
    "s_bitset0_b64 s[92:93], s63\n\t"
    "v_readlane_b32 s64, v27, s63\n\t"
    "s_and_b32 s64, s64, 0xffff0000\n\t"
    "s_call_b64 s[70:71], .Lc_eval_%=\n\t"
    "s_mov_b32 m0, s63\n\t"
    "s_nop 3\n\t"
    "v_writelane_b32 v23, s64, m0\n\t"
    "s_branch .Lc_ms7_%=\n\t"
    ".Lc_mt7_%=:\n\t"
    "s_branch .Lc_m5_%=\n\t"
    ".Lc_eval_%=:\n\t"
    "v_lshlrev_b32 v44, 2, v0\n\t"
    "v_mov_b32 v45, 0\n\t"
    "v_mov_b32 v46, 0xc0135761\n\t"
    "s_mov_b32 s65, 14\n\t"
    ".Lc_ev1_%=:\n\t"
    "ds_read_b32 v48, v44 offset:52736\n\t"
    "ds_read_b32 v49, v44 offset:56832\n\t"
    "ds_read_b32 v50, v44 offset:60928\n\t"
    "v_add_u32 v44, 256, v44\n\t"
    "s_waitcnt lgkmcnt(0)\n\t"
    "v_lshlrev_b32 v52, 16, v48\n\t"
    "v_lshlrev_b32 v53, 16, v49\n\t"
    "v_lshlrev_b32 v54, 16, v50\n\t"
    "v_fma_f32 v52, s64, v52, v53\n\t"
    "v_mul_f32 v53, v52, v52\n\t"
    "v_fmamk_f32 v53, v53, 0xbdd2d3e8, v46\n\t"
    "v_mul_f32 v53, v52, v53\n\t"
    "v_exp_f32 v53, v53\n\t"
    "v_mul_f32 v52, v52, v54\n\t"
    "v_add_f32 v53, 1.0, v53\n\t"
    "v_rcp_f32 v53, v53\n\t"
    "s_nop 0\n\t"
    "v_fmac_f32 v45, v52, v53\n\t"
    "v_and_b32 v52, 0xffff0000, v48\n\t"
    "v_and_b32 v53, 0xffff0000, v49\n\t"
    "v_and_b32 v54, 0xffff0000, v50\n\t"
    "v_fma_f32 v52, s64, v52, v53\n\t"
    "v_mul_f32 v53, v52, v52\n\t"
    "v_fmamk_f32 v53, v53, 0xbdd2d3e8, v46\n\t"
    "v_mul_f32 v53, v52, v53\n\t"
    "v_exp_f32 v53, v53\n\t"
    "v_mul_f32 v52, v52, v54\n\t"
    "v_add_f32 v53, 1.0, v53\n\t"
    "v_rcp_f32 v53, v53\n\t"
    "s_nop 0\n\t"
    "v_fmac_f32 v45, v52, v53\n\t"
    "s_sub_u32 s65, s65, 1\n\t"
    "s_cmp_lg_u32 s65, 0\n\t"
    "s_cbranch_scc1 .Lc_ev1_%=\n\t"
    "s_nop 1\n\t"
    "v_add_f32_dpp v45, v45, v45 quad_perm:[1,0,3,2] row_mask:0xf bank_mask:0xf\n\t"
    "s_nop 1\n\t"
    "v_add_f32_dpp v45, v45, v45 quad_perm:[2,3,0,1] row_mask:0xf bank_mask:0xf\n\t"
    "s_nop 1\n\t"
    "v_add_f32_dpp v45, v45, v45 row_half_mirror row_mask:0xf bank_mask:0xf\n\t"
    "s_nop 1\n\t"
    "v_add_f32_dpp v45, v45, v45 row_mirror row_mask:0xf bank_mask:0xf\n\t"
    "v_mov_b32 v46, v45\n\t"
    "s_nop 1\n\t"
    "v_permlane16_swap_b32 v45, v46\n\t"
    "v_add_f32 v45, v45, v46\n\t"
    "v_mov_b32 v46, v45\n\t"
    "s_nop 1\n\t"
    "v_permlane32_swap_b32 v45, v46\n\t"
    "v_add_f32 v45, v45, v46\n\t"
    "v_add_f32 v45, s96, v45\n\t"
    "s_nop 0\n\t"
    "v_readfirstlane_b32 s64, v45\n\t"
    "s_setpc_b64 s[70:71]\n\t"
    ".Lc_xdma_%=:\n\t"
    "s_cmp_gt_i32 s54, 5\n\t"
    "s_cbranch_scc1 .Lc_xd1_%=\n\t"
    "s_add_u32 s63, s54, 2\n\t"
    "s_andn2_b32 s64, 1, s63\n\t"
    "s_lshl_b32 s64, s64, 15\n\t"
    "s_and_b32 s65, s52, 1\n\t"
    "s_lshl_b32 s61, s65, 14\n\t"
    "s_add_u32 s61, s61, s64\n\t"
    "s_add_u32 s61, s61, 98304\n\t"
    "s_lshl_b32 s63, s63, 3\n\t"
    "s_lshr_b32 s64, s53, 1\n\t"
    "s_add_u32 s63, s63, s64\n\t"
    "s_lshl_b32 s60, s63, 22\n\t"
    "s_lshl_b32 s65, s65, 13\n\t"
    "s_add_u32 s60, s60, s65\n\t"
    "s_add_u32 s60, s60, 4177920\n\t"
    "s_add_u32 s62, s60, 4096\n\t"
    "s_cmp_ge_u32 s52, 2\n\t"
    "s_cbranch_scc1 .Lc_xd2_%=\n\t"
    "v_lshrrev_b32 v60, 5, v0\n\t"
    "v_lshlrev_b32 v60, 1, v60\n\t"
    "v_and_b32 v61, 7, v0\n\t"
    "v_xor_b32 v60, v60, v61\n\t"
    "v_lshlrev_b32 v60, 4, v60\n\t"
    "v_lshrrev_b32 v61, 3, v0\n\t"
    "v_lshl_add_u32 v60, v61, 9, v60\n\t"
    "s_add_u32 m0, s61, 0\n\t"
    "s_nop 0\n\t"
    "buffer_load_dwordx4 v60, s[44:47], s60 offen offset:0 nt lds\n\t"
    "s_add_u32 m0, s61, 896\n\t"
    "s_nop 0\n\t"
    "buffer_load_dwordx4 v60, s[44:47], s60 offen offset:128 nt lds\n\t"
    "s_add_u32 m0, s61, 1792\n\t"
    "s_nop 0\n\t"
    "buffer_load_dwordx4 v60, s[44:47], s60 offen offset:256 nt lds\n\t"
    "s_add_u32 m0, s61, 2688\n\t"
    "s_nop 0\n\t"
    "buffer_load_dwordx4 v60, s[44:47], s60 offen offset:384 nt lds\n\t"
    "s_add_u32 m0, s61, 4096\n\t"
    "s_nop 0\n\t"
    "buffer_load_dwordx4 v60, s[44:47], s62 offen offset:0 nt lds\n\t"
    "s_add_u32 m0, s61, 4992\n\t"
    "s_nop 0\n\t"
    "buffer_load_dwordx4 v60, s[44:47], s62 offen offset:128 nt lds\n\t"
    "s_add_u32 m0, s61, 5888\n\t"
    "s_nop 0\n\t"
    "buffer_load_dwordx4 v60, s[44:47], s62 offen offset:256 nt lds\n\t"
    "s_add_u32 m0, s61, 6784\n\t"
    "s_nop 0\n\t"
    "buffer_load_dwordx4 v60, s[44:47], s62 offen offset:384 nt lds\n\t"
    "s_setpc_b64 s[68:69]\n\t"
    ".Lc_xd2_%=:\n\t"
    "v_and_b32 v61, 15, v0\n\t"
    "v_lshlrev_b32 v61, 4, v61\n\t"
    "v_lshrrev_b32 v62, 4, v0\n\t"
    "v_lshl_add_u32 v61, v62, 9, v61\n\t"
    "s_add_u32 m0, s61, 8192\n\t"
    "s_nop 0\n\t"
    "buffer_load_dwordx4 v61, s[48:51], s60 offen offset:0 nt lds\n\t"
    "s_add_u32 m0, s61, 8960\n\t"
    "s_nop 0\n\t"
    "buffer_load_dwordx4 v61, s[48:51], s60 offen offset:256 nt lds\n\t"
    "s_add_u32 m0, s61, 8192\n\t"
    "s_nop 0\n\t"
    "buffer_load_dwordx4 v61, s[48:51], s60 offen offset:2048 nt lds\n\t"
    "s_add_u32 m0, s61, 8960\n\t"
    "s_nop 0\n\t"
    "buffer_load_dwordx4 v61, s[48:51], s60 offen offset:2304 nt lds\n\t"
    "s_add_u32 m0, s61, 12288\n\t"
    "s_nop 0\n\t"
    "buffer_load_dwordx4 v61, s[48:51], s62 offen offset:0 nt lds\n\t"
    "s_add_u32 m0, s61, 13056\n\t"
    "s_nop 0\n\t"
    "buffer_load_dwordx4 v61, s[48:51], s62 offen offset:256 nt lds\n\t"
    "s_add_u32 m0, s61, 12288\n\t"
    "s_nop 0\n\t"
    "buffer_load_dwordx4 v61, s[48:51], s62 offen offset:2048 nt lds\n\t"
    "s_add_u32 m0, s61, 13056\n\t"
    "s_nop 0\n\t"
    "buffer_load_dwordx4 v61, s[48:51], s62 offen offset:2304 nt lds\n\t"
    ".Lc_xd1_%=:\n\t"
    "s_setpc_b64 s[68:69]\n\t"
    ".Lc_extra_%=:\n\t"
    "s_lshr_b32 s65, s52, 1\n\t"
    "s_add_u32 s56, s54, s65\n\t"
    "s_cmp_gt_u32 s56, 7\n\t"
    "s_cbranch_scc1 .Lc_x9_%=\n\t"
    "s_lshl_b32 s64, s56, 9\n\t"
    "s_add_u32 s64, s64, 24576\n\t"
    "s_cmp_eq_u32 s65, 0\n\t"
    "s_cselect_b32 s97, 28672, s64\n\t"
    "s_add_u32 s63, s54, 1\n\t"
    "s_lshr_b32 s57, s63, 1\n\t"
    "s_add_u32 s57, s57, 1\n\t"
    "s_and_b32 s57, s57, 1\n\t"
    "s_and_b32 s64, s63, 1\n\t"
    "s_lshl_b32 s64, s64, 15\n\t"
    "s_add_u32 s64, s64, s36\n\t"
    "s_add_u32 s62, s64, 679936\n\t"
    "s_lshl_b32 s60, s64, 3\n\t"
    "s_add_u32 s60, s60, 778240\n\t"
    "s_mov_b64 s[92:93], 0xff\n\t"
    "s_cmp_lg_u32 s65, 0\n\t"
    "s_cbranch_scc1 .Lc_x1_%=\n\t"
    "s_cmp_eq_u32 s54, 7\n\t"
    "s_cbranch_scc1 .Lc_x1_%=\n\t"
    "s_mov_b32 s57, 1\n\t"
    "s_mov_b64 s[92:93], 1\n\t"
    "s_lshl_b32 s60, s36, 3\n\t"
    "s_lshl_b32 s63, s54, 18\n\t"
    "s_add_u32 s60, s60, s63\n\t"
    "s_add_u32 s60, s60, 1302528\n\t"
    "s_and_b32 s62, s36, 0xf000\n\t"
    "s_lshl_b32 s63, s54, 4\n\t"
    "s_add_u32 s62, s62, s63\n\t"
    "s_and_b32 s63, s53, 1\n\t"
    "s_lshl_b32 s63, s63, 3\n\t"
    "s_add_u32 s62, s62, s63\n\t"
    "s_add_u32 s62, s62, 745472\n\t"
    ".Lc_x1_%=:\n\t"
    "s_and_b32 s65, s52, 1\n\t"
    "s_lshl_b32 s63, s65, 7\n\t"
    "s_add_u32 s62, s62, s63\n\t"
    "s_add_u32 s62, s62, 3840\n\t"
    "s_lshl_b32 s63, s65, 10\n\t"
    "s_add_u32 s60, s60, s63\n\t"
    "s_add_u32 s60, s60, 30720\n\t"
    "s_andn2_b32 s63, 1, s56\n\t"
    "s_lshl_b32 s63, s63, 15\n\t"
    "s_lshl_b32 s65, s65, 14\n\t"
    "s_add_u32 s63, s63, s65\n\t"
    "s_add_u32 s63, s63, 98304\n\t"
    "v_lshrrev_b32 v1, 4, v0\n\t"
    "v_and_b32 v2, 1, v0\n\t"
    "v_lshlrev_b32 v3, 1, v2\n\t"
    "v_xor_b32 v3, v3, v1\n\t"
    "v_lshlrev_b32 v3, 4, v3\n\t"
    "v_lshl_add_u32 v3, v2, 9, v3\n\t"
    "v_bfe_u32 v4, v0, 2, 2\n\t"
    "v_lshl_add_u32 v3, v4, 7, v3\n\t"
    "v_bfe_u32 v4, v0, 1, 1\n\t"
    "v_lshl_add_u32 v3, v4, 12, v3\n\t"
    "v_add_u32 v3, s63, v3\n\t"
    "v_lshl_add_u32 v4, v1, 4, s97\n\t"
    "v_lshl_add_u32 v5, v0, 4, s63\n\t"
    "ds_read_b128 v[16:19], v4 offset:0\n\t"
    "ds_read_b128 v[20:23], v4 offset:64\n\t"
    "ds_read_b128 v[24:27], v4 offset:128\n\t"
    "ds_read_b128 v[28:31], v4 offset:192\n\t"
    "ds_read_b128 v[32:35], v3 offset:0\n\t"
    "ds_read_b128 v[36:39], v3 offset:64\n\t"
    "ds_read_b128 v[40:43], v3 offset:1024\n\t"
    "ds_read_b128 v[44:47], v3 offset:1088\n\t"
    "ds_read_b128 v[48:51], v3 offset:2048\n\t"
    "ds_read_b128 v[52:55], v3 offset:2112\n\t"
    "ds_read_b128 v[56:59], v3 offset:3072\n\t"
    "ds_read_b128 v[60:63], v3 offset:3136\n\t"
    "v_lshlrev_b32 v2, 4, v2\n\t"
    "v_mov_b32 v6, 0xffff\n\t"
    "v_lshlrev_b32 v6, v2, v6\n\t"
    "v_bfe_u32 v2, v0, 1, 2\n\t"
    "v_cmp_eq_u32 vcc, 0, v2\n\t"
    "s_nop 1\n\t"
    "v_cndmask_b32 v12, 0, v6, vcc\n\t"
    "v_cmp_eq_u32 vcc, 1, v2\n\t"
    "s_nop 1\n\t"
    "v_cndmask_b32 v13, 0, v6, vcc\n\t"
    "v_cmp_eq_u32 vcc, 2, v2\n\t"
    "s_nop 1\n\t"
    "v_cndmask_b32 v14, 0, v6, vcc\n\t"
    "v_cmp_eq_u32 vcc, 3, v2\n\t"
    "s_nop 1\n\t"
    "v_cndmask_b32 v15, 0, v6, vcc\n\t"
    "s_waitcnt lgkmcnt(0)\n\t"
    "v_mfma_f32_16x16x32_bf16 v[8:11], v[32:35], v[16:19], 0\n\t"
    "v_mfma_f32_16x16x32_bf16 v[8:11], v[36:39], v[20:23], v[8:11]\n\t"
    "v_mfma_f32_16x16x32_bf16 v[8:11], v[40:43], v[24:27], v[8:11]\n\t"
    "v_mfma_f32_16x16x32_bf16 v[8:11], v[44:47], v[28:31], v[8:11]\n\t"
    "ds_read_b128 v[16:19], v4 offset:256\n\t"
    "ds_read_b128 v[20:23], v4 offset:320\n\t"
    "ds_read_b128 v[24:27], v4 offset:384\n\t"
    "ds_read_b128 v[28:31], v4 offset:448\n\t"
    "ds_read_b128 v[32:35], v5 offset:8192\n\t"
    "ds_read_b128 v[36:39], v5 offset:9216\n\t"
    "ds_read_b128 v[40:43], v5 offset:10240\n\t"
    "ds_read_b128 v[44:47], v5 offset:11264\n\t"
    "s_waitcnt lgkmcnt(4)\n\t"
    "v_mfma_f32_16x16x32_bf16 v[8:11], v[48:51], v[16:19], v[8:11]\n\t"
    "v_mfma_f32_16x16x32_bf16 v[8:11], v[52:55], v[20:23], v[8:11]\n\t"
    "v_mfma_f32_16x16x32_bf16 v[8:11], v[56:59], v[24:27], v[8:11]\n\t"
    "v_mfma_f32_16x16x32_bf16 v[8:11], v[60:63], v[28:31], v[8:11]\n\t"
    "ds_read_b128 v[48:51], v5 offset:12288\n\t"
    "ds_read_b128 v[52:55], v5 offset:13312\n\t"
    "ds_read_b128 v[56:59], v5 offset:14336\n\t"
    "ds_read_b128 v[60:63], v5 offset:15360\n\t"
    "s_nop 3\n\t"
    "v_max3_f32 v6, v8, v9, v10\n\t"
    "v_max_f32 v6, v6, v11\n\t"
    "v_mov_b32 v2, v6\n\t"
    "s_nop 1\n\t"
    "v_permlane16_swap_b32 v6, v2\n\t"
    "v_max_f32 v6, v6, v2\n\t"
    "v_mov_b32 v2, v6\n\t"
    "s_nop 1\n\t"
    "v_permlane32_swap_b32 v6, v2\n\t"
    "v_max_f32 v6, v6, v2\n\t"
    "v_mul_f32 v6, s66, v6\n\t"
    "v_bfi_b32 v6, 1, s57, v6\n\t"
    "v_fma_f32 v8, v8, s66, -v6\n\t"
    "v_fma_f32 v9, v9, s66, -v6\n\t"
    "v_fma_f32 v10, v10, s66, -v6\n\t"
    "v_fma_f32 v11, v11, s66, -v6\n\t"
    "v_exp_f32 v8, v8\n\t"
    "v_exp_f32 v9, v9\n\t"
    "v_exp_f32 v10, v10\n\t"
    "v_exp_f32 v11, v11\n\t"
    "v_add_f32 v7, v8, v9\n\t"
    "v_add_f32 v7, v7, v10\n\t"
    "v_add_f32 v7, v7, v11\n\t"
    "v_cvt_pk_bf16_f32 v8, v8, v8\n\t"
    "v_cvt_pk_bf16_f32 v9, v9, v9\n\t"
    "v_cvt_pk_bf16_f32 v10, v10, v10\n\t"
    "v_cvt_pk_bf16_f32 v11, v11, v11\n\t"
    "s_waitcnt lgkmcnt(0)\n\t"
    "v_and_b32 v16, v8, v12\n\t"
    "v_and_b32 v17, v8, v13\n\t"
    "v_and_b32 v18, v8, v14\n\t"
    "v_and_b32 v19, v8, v15\n\t"
    "s_nop 1\n\t"
    "v_mfma_f32_16x16x32_bf16 v[24:27], v[16:19], v[32:35], 0\n\t"
    "v_mfma_f32_16x16x32_bf16 v[28:31], v[16:19], v[36:39], 0\n\t"
    "v_and_b32 v20, v9, v12\n\t"
    "v_and_b32 v21, v9, v13\n\t"
    "v_and_b32 v22, v9, v14\n\t"
    "v_and_b32 v23, v9, v15\n\t"
    "s_nop 1\n\t"
    "v_mfma_f32_16x16x32_bf16 v[24:27], v[20:23], v[40:43], v[24:27]\n\t"
    "v_mfma_f32_16x16x32_bf16 v[28:31], v[20:23], v[44:47], v[28:31]\n\t"
    "v_and_b32 v16, v10, v12\n\t"
    "v_and_b32 v17, v10, v13\n\t"
    "v_and_b32 v18, v10, v14\n\t"
    "v_and_b32 v19, v10, v15\n\t"
    "s_nop 1\n\t"
    "v_mfma_f32_16x16x32_bf16 v[24:27], v[16:19], v[48:51], v[24:27]\n\t"
    "v_mfma_f32_16x16x32_bf16 v[28:31], v[16:19], v[52:55], v[28:31]\n\t"
    "v_and_b32 v20, v11, v12\n\t"
    "v_and_b32 v21, v11, v13\n\t"
    "v_and_b32 v22, v11, v14\n\t"
    "v_and_b32 v23, v11, v15\n\t"
    "s_nop 1\n\t"
    "v_mfma_f32_16x16x32_bf16 v[24:27], v[20:23], v[56:59], v[24:27]\n\t"
    "v_mfma_f32_16x16x32_bf16 v[28:31], v[20:23], v[60:63], v[28:31]\n\t"
    "v_mov_b32 v2, v7\n\t"
    "s_nop 1\n\t"
    "v_permlane16_swap_b32 v7, v2\n\t"
    "v_add_f32 v7, v7, v2\n\t"
    "v_mov_b32 v2, v7\n\t"
    "s_nop 1\n\t"
    "v_permlane32_swap_b32 v7, v2\n\t"
    "v_add_f32 v7, v7, v2\n\t"
    "v_bfi_b32 v7, 1, s57, v7\n\t"
    "v_and_b32 v2, 15, v0\n\t"
    "v_lshlrev_b32 v2, 4, v2\n\t"
    "v_lshl_add_u32 v2, v1, 3, v2\n\t"
    "v_cvt_pk_bf16_f32 v24, v24, v25\n\t"
    "v_bfi_b32 v24, 1, s57, v24\n\t"
    "v_cvt_pk_bf16_f32 v25, v26, v27\n\t"
    "v_bfi_b32 v25, 1, s57, v25\n\t"
    "v_cvt_pk_bf16_f32 v26, v28, v29\n\t"
    "v_bfi_b32 v26, 1, s57, v26\n\t"
    "v_cvt_pk_bf16_f32 v27, v30, v31\n\t"
    "v_bfi_b32 v27, 1, s57, v27\n\t"
    "v_lshlrev_b32 v3, 3, v0\n\t"
    "s_mov_b32 exec_hi, 0\n\t"
    "buffer_store_dwordx2 v[24:25], v2, s[40:43], s60 offen sc1\n\t"
    "buffer_store_dwordx2 v[26:27], v2, s[40:43], s60 offen offset:256 sc1\n\t"
    "s_mov_b64 exec, s[92:93]\n\t"
    "s_cmp_lg_u32 s92, 1\n\t"
    "s_cbranch_scc1 .Lc_x2_%=\n\t"
    "s_waitcnt vmcnt(0)\n\t"
    ".Lc_x2_%=:\n\t"
    "buffer_store_dwordx2 v[6:7], v3, s[40:43], s62 offen sc1\n\t"
    "s_mov_b64 exec, -1\n\t"
    ".Lc_x9_%=:\n\t"
    "s_setpc_b64 s[68:69]\n\t"
      :
      : "s"(p.ws), "s"(p.x), "s"(p.wq), "s"(p.wo), "s"(p.K), "s"(p.V), "s"(p.w1), "s"(p.b1), "s"(p.w2), "s"(p.b2), "s"(p.out),
        "s"(h), "s"(w), "s"(profoff)
      :
      "v255", "a255", "s33", "s34", "s35", "s36", "s37", "s38", "s39", "s40", "s41", "s42", "s43", "s44", "s45", "s46",
      "s47", "s48", "s49", "s50", "s51", "s52", "s53", "s54", "s55", "s56", "s57", "s58", "s59", "s60", "s61", "s62",
      "s63", "s64", "s65", "s66", "s67", "s68", "s69", "s70", "s71", "s72", "s73", "s74", "s75", "s76", "s77", "s78",
      "s79", "s80", "s81", "s82", "s83", "s84", "s85", "s86", "s87", "s88", "s89", "s90", "s91", "s92", "s93", "s94",
      "s95", "s96", "s97", "s98", "s99", "vcc", "scc", "memory");
}

__global__ void __launch_bounds__(256, 1) fused_decode(Params p) {
  __shared__ __attribute__((aligned(16))) unsigned char smem[163840];
  asm volatile("" ::"s"((u32)(size_t)(LAS unsigned char*)smem));
  if (blockIdx.x < NCHAIN) {
    if (!STANDALONE) chain_role(p);
  } else {
    stream_role(p);
  }
}

extern "C" void kernel_launch(void* const* d_in, const int* in_sizes, int n_in, void* d_out, int out_size, void* d_ws,
                              size_t ws_size, hipStream_t stream) {
  Params p;
  p.x = (const u16*)d_in[0]; p.K = (const u16*)d_in[1]; p.V = (const u16*)d_in[2];
  p.wq = (const u16*)d_in[3]; p.wo = (const u16*)d_in[4];
  p.w1 = (const u16*)d_in[6]; p.b1 = (const u16*)d_in[7]; p.w2 = (const u16*)d_in[8]; p.b2 = (const u16*)d_in[9];
  p.out = (u16*)d_out; p.ws = (unsigned char*)d_ws;
  hipLaunchKernelGGL(fused_decode, dim3(256), dim3(256), 0, stream, p);
}
```
